```python
import torch

def _softmax(x, axis=-1):
    return torch.softmax(x, dim=axis)
def _gelu(x):
    return torch.nn.functional.gelu(x, approximate="tanh")
def _silu(x):
    return torch.nn.functional.silu(x)
def _sigmoid(x):
    return torch.sigmoid(x)
def _layernorm(x, axis=-1, eps=1e-5):
    m = x.mean(dim=axis, keepdim=True)
    v = x.var(dim=axis, keepdim=True, unbiased=False)
    return (x - m) / torch.sqrt(v + eps)
def _scatter_add(tgt, idx, src, axis=0):
    return tgt.clone().index_add_(axis, idx, src)
def _segment_sum(src, seg, n):
    out = torch.zeros((n,) + src.shape[1:], dtype=src.dtype)
    return out.index_add_(0, seg, src)
def _pad(x, pw):
    flat = tuple(v for pair in reversed(pw) for v in pair)
    return torch.nn.functional.pad(x, flat)

kernel_name = 'recurrent_scan__mha_block_cached__geglu_ffn__decode'

def setup_inputs():

    _w = (lambda a: (lambda t: t.to(torch.bfloat16) if t.is_floating_point() else t)(torch.as_tensor(a)))
    def _A_setup():
        B, Sq, D = 2, 1, 512
        H, Sk, Dh = 32, 16384, 128
        return {
            "x": _w((lambda *s: torch.randn(*s, dtype=torch.bfloat16) * 0.05)(B, Sq, D)),
            "k_cache": _w((lambda *s: torch.randn(*s, dtype=torch.bfloat16) * 1.6)(B, H, Sk, Dh)),
            "v_cache": _w((lambda *s: torch.randn(*s, dtype=torch.bfloat16) * 0.2)(B, H, Sk, Dh)),
            "wq": _w((lambda *s: torch.randn(*s, dtype=torch.bfloat16) * 1.6)(D, H * Dh) * (D ** -0.5)),
            "wo": _w((lambda *s: torch.randn(*s, dtype=torch.bfloat16) * 0.2)(H * Dh, D) * ((H * Dh) ** -0.5)),
        }
    _A_in = _A_setup()

    def _B_setup():
        B, S, D, F = 2, 1, 512, 1376
        return {
            "x": _w((lambda *s: torch.randn(*s, dtype=torch.bfloat16) * 0.05)(B, S, D)),
            "w_gate": _w((lambda *s: torch.randn(*s, dtype=torch.bfloat16) * 0.1)(D, F)),
            "w_up": _w((lambda *s: torch.randn(*s, dtype=torch.bfloat16) * 0.1)(D, F)),
            "w_down": _w((lambda *s: torch.randn(*s, dtype=torch.bfloat16) * 0.1)(F, D)),
        }
    _B_in = _B_setup()

    return {
        "A_x": _A_in["x"],
        "A_k_cache": _A_in["k_cache"],
        "A_v_cache": _A_in["v_cache"],
        "A_wq": _A_in["wq"],
        "A_wo": _A_in["wo"],
        "B_x": _B_in["x"],
        "B_w_gate": _B_in["w_gate"],
        "B_w_up": _B_in["w_up"],
        "B_w_down": _B_in["w_down"],
    }

def reference(A_x, A_k_cache, A_v_cache, A_wq, A_wo, B_x, B_w_gate, B_w_up, B_w_down):
    def _A_ref(x, k_cache, v_cache, wq, wo):
        B, Sq, D = x.shape
        H, Dh = 32, 128
        q = (lambda x, *d: x.permute(*d))((x @ wq).reshape(B, Sq, H, Dh), 0, 2, 1, 3)
        scores = torch.einsum("bhqd,bhkd->bhqk", q, k_cache) * (Dh ** -0.5)
        p = _softmax(scores, axis=-1)
        o = torch.einsum("bhqk,bhkd->bhqd", p, v_cache)
        return (lambda x, *d: x.permute(*d))(o, 0, 2, 1, 3).reshape(B, Sq, H * Dh) @ wo

    def _B_ref(x, w_gate, w_up, w_down):
        return (_gelu(x @ w_gate) * (x @ w_up)) @ w_down

    x = A_x
    xn = _layernorm(x, axis=-1)
    s = _A_ref(xn, A_k_cache, A_v_cache, A_wq, A_wo)
    g = _silu(_B_ref(xn, B_w_gate, B_w_up, B_w_down))
    return x + s * g
```

```cpp
#include <hip/hip_runtime.h>
#include <hip/hip_bf16.h>
#include <stdint.h>

typedef unsigned int u32;
typedef __attribute__((ext_vector_type(4))) unsigned int u32x4;
typedef __attribute__((ext_vector_type(4))) float f32x4;
typedef __attribute__((ext_vector_type(2))) float f32x2;
typedef __attribute__((ext_vector_type(2))) __bf16 bf16x2;

constexpr int NB = 2, DM = 512, NH = 32, SK = 16384, DH = 128, FF = 1376;
constexpr int NSP = 4;
constexpr int NW = 8;
constexpr int NT = NW * 64;
constexpr int TOK_WG = SK / NSP;
constexpr int TILE = 16;
constexpr int NTILE = TOK_WG / (TILE * NW);
#define DEPTH 2
constexpr int NGRP = NTILE / DEPTH;
constexpr int FSL = 32;
constexpr int NFS = FF / FSL;
constexpr int NWG = NB * NH * NSP;

constexpr size_t WS_Y = 0;
constexpr size_t WS_ML = WS_Y + (size_t)NWG * DM;
constexpr size_t WS_Z = WS_ML + (size_t)NWG * 2;

__device__ __forceinline__ float bflo(u32 v) { return __builtin_bit_cast(float, v << 16); }
__device__ __forceinline__ float bfhi(u32 v) { return __builtin_bit_cast(float, v & 0xffff0000u); }
__device__ __forceinline__ u32 cvtpk(float lo, float hi) {
  u32 r; asm("v_cvt_pk_bf16_f32 %0, %1, %2" : "=v"(r) : "v"(lo), "v"(hi)); return r;
}
__device__ __forceinline__ float rbf(float x) { return bflo(cvtpk(x, x)); }
__device__ __forceinline__ float dot2(u32 k, u32 q, float acc) {
  return __builtin_amdgcn_fdot2_f32_bf16(__builtin_bit_cast(bf16x2, k), __builtin_bit_cast(bf16x2, q), acc, false);
}
template <int CTRL> __device__ __forceinline__ float dpp(float x) {
  return __builtin_bit_cast(float, __builtin_amdgcn_mov_dpp(__builtin_bit_cast(int, x), CTRL, 0xf, 0xf, true));
}
constexpr int XOR1 = 0xB1, XOR2 = 0x4E, XOR7 = 0x141, XOR8 = 0x128, ROR4 = 0x124;
__device__ __forceinline__ float xrow_sum(float x) {
  auto s = __builtin_amdgcn_permlane16_swap(__float_as_uint(x), __float_as_uint(x), false, false);
  x = __uint_as_float(s[0]) + __uint_as_float(s[1]);
  auto t = __builtin_amdgcn_permlane32_swap(__float_as_uint(x), __float_as_uint(x), false, false);
  return __uint_as_float(t[0]) + __uint_as_float(t[1]);
}
__device__ __forceinline__ float xrow_max(float x) {
  auto s = __builtin_amdgcn_permlane16_swap(__float_as_uint(x), __float_as_uint(x), false, false);
  x = fmaxf(__uint_as_float(s[0]), __uint_as_float(s[1]));
  auto t = __builtin_amdgcn_permlane32_swap(__float_as_uint(x), __float_as_uint(x), false, false);
  return fmaxf(__uint_as_float(t[0]), __uint_as_float(t[1]));
}
__device__ __forceinline__ float row16_sum(float x) {
  x += dpp<XOR1>(x); x += dpp<XOR2>(x); x += dpp<ROR4>(x); x += dpp<XOR8>(x); return x;
}
__device__ __forceinline__ float wave_sum(float x) { return xrow_sum(row16_sum(x)); }
__device__ __forceinline__ u32x4 ldnt(const void* p) { return __builtin_nontemporal_load(reinterpret_cast<const u32x4*>(p)); }
__device__ __forceinline__ u32x4 ld16(const void* p) { return *reinterpret_cast<const u32x4*>(p); }

constexpr int L_XN = 0;
constexpr int L_QP = L_XN + 2 * DM;
constexpr int L_MRG = L_QP + NW * DH;
constexpr int L_ACC = L_MRG + 64;
constexpr int L_RED = L_ACC + NW * DH;
constexpr int L_HH = L_RED + NW * 2 * DM;
constexpr int L_END = L_HH + 64;

__device__ __forceinline__ float gelu_tanh(float x) {
  const float k0 = 0.7978845608028654f, k1 = 0.044715f;
  float u = k0 * (x + k1 * x * x * x);
  return 0.5f * x * (1.0f + tanhf(u));
}

__global__ void __launch_bounds__(NT, 2) k1_main(const __hip_bfloat16* __restrict__ x, const __hip_bfloat16* __restrict__ kc,
                                                  const __hip_bfloat16* __restrict__ vc, const __hip_bfloat16* __restrict__ wq,
                                                  const __hip_bfloat16* __restrict__ wo, const __hip_bfloat16* __restrict__ wg,
                                                  const __hip_bfloat16* __restrict__ wu, const __hip_bfloat16* __restrict__ wd,
                                                  float* __restrict__ ws) {
  __shared__ __attribute__((aligned(16))) float lds[L_END];
  const int tid = threadIdx.x, lane = tid & 63, wave = __builtin_amdgcn_readfirstlane(tid >> 6);
  const int r = lane >> 4, c = lane & 15;
  const int bid = blockIdx.x;
  const int xcd = bid & 7, j = bid >> 3;
  const int h = xcd * 4 + (j >> 3), b = (j >> 2) & 1, sp = j & 3;
  const bool do_ffn = bid < NFS;

  const u32x4 x0 = ld16(x + lane * 8), x1 = ld16(x + DM + lane * 8);
  u32x4 wqr[16];
  {
    const __hip_bfloat16* p = wq + (size_t)(wave * 64 + r * 16) * (NH * DH) + h * DH + c * 8;
#pragma unroll
    for (int i = 0; i < 16; ++i) wqr[i] = ld16(p + (size_t)i * (NH * DH));
  }
  const size_t kvbase = ((size_t)(b * NH + h) * SK + (size_t)sp * TOK_WG + (size_t)wave * TILE) * DH + lane * 8;
  const __hip_bfloat16* kp = kc + kvbase;
  const __hip_bfloat16* vp = vc + kvbase;
  constexpr size_t TSTR = (size_t)NW * TILE * DH;
  u32x4 kb[DEPTH][4], vb[DEPTH][4];
#pragma unroll
  for (int s = 0; s < DEPTH; ++s) {
#pragma unroll
    for (int t = 0; t < 4; ++t) kb[s][t] = ldnt(kp + s * TSTR + t * 512);
#pragma unroll
    for (int t = 0; t < 4; ++t) vb[s][t] = ldnt(vp + s * TSTR + t * 512);
  }

  {
#pragma unroll
    for (int bb = 0; bb < 2; ++bb) {
      const u32x4 xv = bb ? x1 : x0;
      float e[8];
#pragma unroll
      for (int k = 0; k < 4; ++k) { e[2 * k] = bflo(xv[k]); e[2 * k + 1] = bfhi(xv[k]); }
      float s = 0.f;
#pragma unroll
      for (int k = 0; k < 8; ++k) s += e[k];
      const float mean = wave_sum(s) * (1.0f / DM);
      float q2 = 0.f;
#pragma unroll
      for (int k = 0; k < 8; ++k) { const float d = e[k] - mean; q2 = fmaf(d, d, q2); }
      const float var = rbf(wave_sum(q2) * (1.0f / DM));
      const float mb = rbf(mean);
      const float den = rbf(sqrtf(rbf(var + 1e-5f)));
      float o[8];
#pragma unroll
      for (int k = 0; k < 8; ++k) o[k] = rbf(rbf(e[k] - mb) / den);
      if (wave == 0) {
        *reinterpret_cast<f32x4*>(lds + L_XN + bb * DM + lane * 8) = f32x4{o[0], o[1], o[2], o[3]};
        *reinterpret_cast<f32x4*>(lds + L_XN + bb * DM + lane * 8 + 4) = f32x4{o[4], o[5], o[6], o[7]};
      }
    }
  }
  __syncthreads();
  u32 qpk[4];
  {
    float a[8];
#pragma unroll
    for (int k = 0; k < 8; ++k) a[k] = 0.f;
    const float* xs = lds + L_XN + b * DM + wave * 64 + r * 16;
#pragma unroll
    for (int i4 = 0; i4 < 4; ++i4) {
      const f32x4 xv = *reinterpret_cast<const f32x4*>(xs + i4 * 4);
#pragma unroll
      for (int ii = 0; ii < 4; ++ii) {
        const u32x4 w = wqr[i4 * 4 + ii];
#pragma unroll
        for (int k = 0; k < 4; ++k) { a[2 * k] = fmaf(xv[ii], bflo(w[k]), a[2 * k]); a[2 * k + 1] = fmaf(xv[ii], bfhi(w[k]), a[2 * k + 1]); }
      }
    }
#pragma unroll
    for (int k = 0; k < 8; ++k) a[k] = xrow_sum(a[k]);
    if (r == 0) {
      *reinterpret_cast<f32x4*>(lds + L_QP + wave * DH + c * 8) = f32x4{a[0], a[1], a[2], a[3]};
      *reinterpret_cast<f32x4*>(lds + L_QP + wave * DH + c * 8 + 4) = f32x4{a[4], a[5], a[6], a[7]};
    }
    __syncthreads();
    f32x4 s0 = {0.f, 0.f, 0.f, 0.f}, s1 = {0.f, 0.f, 0.f, 0.f};
#pragma unroll
    for (int w = 0; w < NW; ++w) {
      s0 += *reinterpret_cast<const f32x4*>(lds + L_QP + w * DH + c * 8);
      s1 += *reinterpret_cast<const f32x4*>(lds + L_QP + w * DH + c * 8 + 4);
    }
    qpk[0] = cvtpk(s0[0], s0[1]); qpk[1] = cvtpk(s0[2], s0[3]); qpk[2] = cvtpk(s1[0], s1[1]); qpk[3] = cvtpk(s1[2], s1[3]);
  }

  constexpr float SCALE = 0.08838834764831845f;
  constexpr float LOG2E = 1.4426950408889634f;
  float m = -INFINITY, l = 0.f, acc[8];
#pragma unroll
  for (int k = 0; k < 8; ++k) acc[k] = 0.f;
  const bool b0 = (c & 1) != 0, b1 = (c & 2) != 0;

  auto process = [&](u32x4 (&K)[4], u32x4 (&V)[4], const __hip_bfloat16* kn, const __hip_bfloat16* vn, bool reload) {
    float d[4];
#pragma unroll
    for (int t = 0; t < 4; ++t) {
      float s = dot2(K[t][0], qpk[0], 0.f);
      s = dot2(K[t][1], qpk[1], s); s = dot2(K[t][2], qpk[2], s); s = dot2(K[t][3], qpk[3], s);
      d[t] = s;
    }
    if (reload) {
#pragma unroll
      for (int t = 0; t < 4; ++t) K[t] = ldnt(kn + t * 512);
    }
    float e0 = b0 ? d[1] : d[0], f0 = b0 ? d[0] : d[1];
    float e1 = b0 ? d[3] : d[2], f1 = b0 ? d[2] : d[3];
    e0 += dpp<XOR1>(f0); e1 += dpp<XOR1>(f1);
    float g0 = b1 ? e1 : e0, g1 = b1 ? e0 : e1;
    g0 += dpp<XOR2>(g1);
    g0 += dpp<ROR4>(g0); g0 += dpp<XOR8>(g0);
    const float sc = rbf(rbf(g0) * SCALE) * LOG2E;
    float tm = fmaxf(sc, dpp<XOR1>(sc)); tm = fmaxf(tm, dpp<XOR2>(tm));
    const float mn = fmaxf(m, tm);
    const float alpha = __builtin_amdgcn_exp2f(m - mn), p = __builtin_amdgcn_exp2f(sc - mn);
    m = mn;
    l = fmaf(l, alpha, p);
    const float p0 = dpp<0x00>(p), p1 = dpp<0x55>(p), p2 = dpp<0xAA>(p), p3 = dpp<0xFF>(p);
#pragma unroll
    for (int k = 0; k < 8; ++k) acc[k] *= alpha;
#pragma unroll
    for (int t = 0; t < 4; ++t) {
      const float pt = t == 0 ? p0 : t == 1 ? p1 : t == 2 ? p2 : p3;
#pragma unroll
      for (int k = 0; k < 4; ++k) { acc[2 * k] = fmaf(pt, bflo(V[t][k]), acc[2 * k]); acc[2 * k + 1] = fmaf(pt, bfhi(V[t][k]), acc[2 * k + 1]); }
    }
    if (reload) {
#pragma unroll
      for (int t = 0; t < 4; ++t) V[t] = ldnt(vn + t * 512);
    }
  };

  u32x4 fg[4], fu[4], fd[4];
  if (do_ffn) {
    const int f0 = bid * FSL;
    const int rr = lane >> 2, cc = lane & 3;
    const size_t off = (size_t)(wave * 64 + rr * 4) * FF + f0 + cc * 8;
#pragma unroll
    for (int i = 0; i < 4; ++i) { fg[i] = ld16(wg + off + (size_t)i * FF); fu[i] = ld16(wu + off + (size_t)i * FF); }
#pragma unroll
    for (int i = 0; i < 4; ++i) fd[i] = ld16(wd + (size_t)(f0 + wave * 4 + i) * DM + lane * 8);
  }

#pragma unroll
  for (int s = 0; s < DEPTH; ++s) process(kb[s], vb[s], kp + (DEPTH + s) * TSTR, vp + (DEPTH + s) * TSTR, true);

  if (do_ffn) {
    const int rr = lane >> 2;
    float ag[2][8], au[2][8];
#pragma unroll
    for (int bb = 0; bb < 2; ++bb) {
      const f32x4 xv = *reinterpret_cast<const f32x4*>(lds + L_XN + bb * DM + wave * 64 + rr * 4);
#pragma unroll
      for (int k = 0; k < 8; ++k) { ag[bb][k] = 0.f; au[bb][k] = 0.f; }
#pragma unroll
      for (int i = 0; i < 4; ++i)
#pragma unroll
        for (int k = 0; k < 4; ++k) {
          ag[bb][2 * k] = fmaf(xv[i], bflo(fg[i][k]), ag[bb][2 * k]); ag[bb][2 * k + 1] = fmaf(xv[i], bfhi(fg[i][k]), ag[bb][2 * k + 1]);
          au[bb][2 * k] = fmaf(xv[i], bflo(fu[i][k]), au[bb][2 * k]); au[bb][2 * k + 1] = fmaf(xv[i], bfhi(fu[i][k]), au[bb][2 * k + 1]);
        }
#pragma unroll
      for (int k = 0; k < 8; ++k) {
        float t = ag[bb][k]; t += dpp<ROR4>(t); t += dpp<XOR8>(t); ag[bb][k] = xrow_sum(t);
        float u = au[bb][k]; u += dpp<ROR4>(u); u += dpp<XOR8>(u); au[bb][k] = xrow_sum(u);
      }
    }
    if (lane < 4) {
#pragma unroll
      for (int bb = 0; bb < 2; ++bb) {
        float* pg = lds + L_RED + ((wave * 2 + 0) * 2 + bb) * 32 + lane * 8;
        float* pu = lds + L_RED + ((wave * 2 + 1) * 2 + bb) * 32 + lane * 8;
        *reinterpret_cast<f32x4*>(pg) = f32x4{ag[bb][0], ag[bb][1], ag[bb][2], ag[bb][3]};
        *reinterpret_cast<f32x4*>(pg + 4) = f32x4{ag[bb][4], ag[bb][5], ag[bb][6], ag[bb][7]};
        *reinterpret_cast<f32x4*>(pu) = f32x4{au[bb][0], au[bb][1], au[bb][2], au[bb][3]};
        *reinterpret_cast<f32x4*>(pu + 4) = f32x4{au[bb][4], au[bb][5], au[bb][6], au[bb][7]};
      }
    }
    __syncthreads();
    if (tid < 64) {
      float g = 0.f, u = 0.f;
#pragma unroll
      for (int w = 0; w < NW; ++w) { g += lds[L_RED + (w * 2 + 0) * 64 + tid]; u += lds[L_RED + (w * 2 + 1) * 64 + tid]; }
      const float gl = rbf(gelu_tanh(rbf(g)));
      lds[L_HH + tid] = rbf(gl * rbf(u));
    }
    __syncthreads();
    float z[2][8];
#pragma unroll
    for (int bb = 0; bb < 2; ++bb) {
      const f32x4 hv = *reinterpret_cast<const f32x4*>(lds + L_HH + bb * 32 + wave * 4);
#pragma unroll
      for (int k = 0; k < 8; ++k) z[bb][k] = 0.f;
#pragma unroll
      for (int i = 0; i < 4; ++i)
#pragma unroll
        for (int k = 0; k < 4; ++k) { z[bb][2 * k] = fmaf(hv[i], bflo(fd[i][k]), z[bb][2 * k]); z[bb][2 * k + 1] = fmaf(hv[i], bfhi(fd[i][k]), z[bb][2 * k + 1]); }
      float* pz = lds + L_RED + (wave * 2 + bb) * DM + lane * 8;
      *reinterpret_cast<f32x4*>(pz) = f32x4{z[bb][0], z[bb][1], z[bb][2], z[bb][3]};
      *reinterpret_cast<f32x4*>(pz + 4) = f32x4{z[bb][4], z[bb][5], z[bb][6], z[bb][7]};
    }
    __syncthreads();
    {
      f32x2 s = {0.f, 0.f};
#pragma unroll
      for (int w = 0; w < NW; ++w) s += *reinterpret_cast<const f32x2*>(lds + L_RED + w * 2 * DM + tid * 2);
      *reinterpret_cast<f32x2*>(ws + WS_Z + (size_t)bid * 2 * DM + tid * 2) = s;
    }
    __syncthreads();
  }

  for (int g = 1; g < NGRP - 1; ++g) {
    const __hip_bfloat16* kn = kp + (size_t)(g + 1) * DEPTH * TSTR;
    const __hip_bfloat16* vn = vp + (size_t)(g + 1) * DEPTH * TSTR;
#pragma unroll
    for (int s = 0; s < DEPTH; ++s) process(kb[s], vb[s], kn + s * TSTR, vn + s * TSTR, true);
  }
  u32x4 wor[16];
  {
    const __hip_bfloat16* p = wo + (size_t)(h * DH + wave * 16) * DM + lane * 8;
#pragma unroll
    for (int s = 0; s < DEPTH; ++s) {
      process(kb[s], vb[s], nullptr, nullptr, false);
#pragma unroll
      for (int i = s * (16 / DEPTH); i < (s + 1) * (16 / DEPTH); ++i) wor[i] = ld16(p + (size_t)i * DM);
    }
  }

  l += dpp<XOR1>(l); l += dpp<XOR2>(l);
  {
    const float mw = xrow_max(m);
    const float sc = __builtin_amdgcn_exp2f(m - mw);
    l = xrow_sum(l * sc);
#pragma unroll
    for (int k = 0; k < 8; ++k) acc[k] = xrow_sum(acc[k] * sc);
    if (r == 0) {
      *reinterpret_cast<f32x4*>(lds + L_ACC + wave * DH + c * 8) = f32x4{acc[0], acc[1], acc[2], acc[3]};
      *reinterpret_cast<f32x4*>(lds + L_ACC + wave * DH + c * 8 + 4) = f32x4{acc[4], acc[5], acc[6], acc[7]};
      if (c == 0) { lds[L_MRG + wave * 2] = mw; lds[L_MRG + wave * 2 + 1] = l; }
    }
  }
  __syncthreads();
  {
    float M = -INFINITY;
#pragma unroll
    for (int w = 0; w < NW; ++w) M = fmaxf(M, lds[L_MRG + w * 2]);
    float L = 0.f, od = 0.f;
#pragma unroll
    for (int w = 0; w < NW; ++w) {
      const float wt = __builtin_amdgcn_exp2f(lds[L_MRG + w * 2] - M);
      L = fmaf(wt, lds[L_MRG + w * 2 + 1], L);
      od = fmaf(wt, lds[L_ACC + w * DH + wave * 16 + c], od);
    }
    float y[8];
#pragma unroll
    for (int k = 0; k < 8; ++k) y[k] = 0.f;
#pragma unroll
    for (int i = 0; i < 16; ++i) {
      const float oi = __builtin_bit_cast(float, __builtin_amdgcn_readlane(__builtin_bit_cast(int, od), i));
#pragma unroll
      for (int k = 0; k < 4; ++k) { y[2 * k] = fmaf(oi, bflo(wor[i][k]), y[2 * k]); y[2 * k + 1] = fmaf(oi, bfhi(wor[i][k]), y[2 * k + 1]); }
    }
    float* py = lds + L_RED + wave * DM + lane * 8;
    *reinterpret_cast<f32x4*>(py) = f32x4{y[0], y[1], y[2], y[3]};
    *reinterpret_cast<f32x4*>(py + 4) = f32x4{y[4], y[5], y[6], y[7]};
    __syncthreads();
    float s = 0.f;
#pragma unroll
    for (int w = 0; w < NW; ++w) s += lds[L_RED + w * DM + tid];
    const int slot = (b * NH + h) * NSP + sp;
    ws[WS_Y + (size_t)slot * DM + tid] = s;
    if (tid == 0) *reinterpret_cast<f32x2*>(ws + WS_ML + slot * 2) = f32x2{M, L};
  }
}

__global__ void __launch_bounds__(128) k2_final(const __hip_bfloat16* __restrict__ x, const float* __restrict__ ws, __hip_bfloat16* __restrict__ out) {
  __shared__ float red[2][2][16];
  const int tid = threadIdx.x, lane = tid & 63, wave = tid >> 6;
  const int b = blockIdx.x >> 5, cg = blockIdx.x & 31;
  const int slot = b * NH * NSP + tid;
  const f32x4* py = reinterpret_cast<const f32x4*>(ws + WS_Y + (size_t)slot * DM + cg * 16);
  f32x4 y[4], z[4];
#pragma unroll
  for (int i = 0; i < 4; ++i) y[i] = py[i];
  const f32x2 ml = *reinterpret_cast<const f32x2*>(ws + WS_ML + slot * 2);
  if (tid < NFS) {
    const f32x4* pz = reinterpret_cast<const f32x4*>(ws + WS_Z + (size_t)(tid * 2 + b) * DM + cg * 16);
#pragma unroll
    for (int i = 0; i < 4; ++i) z[i] = pz[i];
  } else {
#pragma unroll
    for (int i = 0; i < 4; ++i) z[i] = f32x4{0.f, 0.f, 0.f, 0.f};
  }
  float M = fmaxf(ml[0], dpp<XOR1>(ml[0])); M = fmaxf(M, dpp<XOR2>(M));
  const float e = __builtin_amdgcn_exp2f(ml[0] - M);
  float L = e * ml[1]; L += dpp<XOR1>(L); L += dpp<XOR2>(L);
  const float wt = e / L;
  float sy[16], sz[16];
#pragma unroll
  for (int i = 0; i < 4; ++i)
#pragma unroll
    for (int k = 0; k < 4; ++k) { sy[i * 4 + k] = wave_sum(y[i][k] * wt); sz[i * 4 + k] = wave_sum(z[i][k]); }
  if (lane == 0) {
#pragma unroll
    for (int k = 0; k < 16; ++k) { red[wave][0][k] = sy[k]; red[wave][1][k] = sz[k]; }
  }
  __syncthreads();
  if (tid < 16) {
    const float s = rbf(red[0][0][tid] + red[1][0][tid]);
    const float f = rbf(red[0][1][tid] + red[1][1][tid]);
    const float g = rbf(f / (1.0f + __expf(-f)));
    const float xv = __bfloat162float(x[b * DM + cg * 16 + tid]);
    out[b * DM + cg * 16 + tid] = __float2bfloat16(xv + rbf(s * g));
  }
}

extern "C" void kernel_launch(void* const* d_in, const int* in_sizes, int n_in, void* d_out, int out_size, void* d_ws, size_t ws_size,
                              hipStream_t stream) {
  const __hip_bfloat16* x = (const __hip_bfloat16*)d_in[0];
  const __hip_bfloat16* kc = (const __hip_bfloat16*)d_in[1];
  const __hip_bfloat16* vc = (const __hip_bfloat16*)d_in[2];
  const __hip_bfloat16* wq = (const __hip_bfloat16*)d_in[3];
  const __hip_bfloat16* wo = (const __hip_bfloat16*)d_in[4];
  const __hip_bfloat16* wg = (const __hip_bfloat16*)d_in[6];
  const __hip_bfloat16* wu = (const __hip_bfloat16*)d_in[7];
  const __hip_bfloat16* wd = (const __hip_bfloat16*)d_in[8];
  float* ws = (float*)d_ws;
  k1_main<<<NWG, NT, 0, stream>>>(x, kc, vc, wq, wo, wg, wu, wd, ws);
  k2_final<<<NB * 32, 128, 0, stream>>>(x, ws, (__hip_bfloat16*)d_out);
}
```

```cpp
#include <hip/hip_runtime.h>
#include <hip/hip_bf16.h>
#include <stdint.h>

typedef unsigned int u32;
typedef __attribute__((ext_vector_type(4))) unsigned int u32x4;
typedef __attribute__((ext_vector_type(4))) float f32x4;
typedef __attribute__((ext_vector_type(2))) float f32x2;
typedef __attribute__((ext_vector_type(2))) __bf16 bf16x2;

constexpr int NB = 2, DM = 512, NH = 32, SK = 16384, DH = 128, FF = 1376;
constexpr int NSP = 4;
constexpr int NW = 8;
constexpr int NT = NW * 64;
constexpr int TOK_WG = SK / NSP;
constexpr int TILE = 16;
constexpr int NTILE = TOK_WG / (TILE * NW);
constexpr int NTWG = NTILE * NW;
constexpr int FSL = 32;
constexpr int NFS = FF / FSL;
constexpr int NWG = NB * NH * NSP;

constexpr size_t WS_Y = 0;
constexpr size_t WS_ML = WS_Y + (size_t)NWG * DM;
constexpr size_t WS_Z = WS_ML + (size_t)NWG * 2;

__device__ __forceinline__ float bflo(u32 v) { return __builtin_bit_cast(float, v << 16); }
__device__ __forceinline__ float bfhi(u32 v) { return __builtin_bit_cast(float, v & 0xffff0000u); }
__device__ __forceinline__ u32 cvtpk(float lo, float hi) {
  u32 r; asm("v_cvt_pk_bf16_f32 %0, %1, %2" : "=v"(r) : "v"(lo), "v"(hi)); return r;
}
__device__ __forceinline__ float rbf(float x) { return bflo(cvtpk(x, x)); }
__device__ __forceinline__ float dot2(u32 k, u32 q, float acc) {
  return __builtin_amdgcn_fdot2_f32_bf16(__builtin_bit_cast(bf16x2, k), __builtin_bit_cast(bf16x2, q), acc, false);
}
template <int CTRL> __device__ __forceinline__ float dpp(float x) {
  return __builtin_bit_cast(float, __builtin_amdgcn_mov_dpp(__builtin_bit_cast(int, x), CTRL, 0xf, 0xf, true));
}
constexpr int XOR1 = 0xB1, XOR2 = 0x4E, XOR7 = 0x141, XOR8 = 0x128, ROR4 = 0x124;
__device__ __forceinline__ float xrow_sum(float x) {
  auto s = __builtin_amdgcn_permlane16_swap(__float_as_uint(x), __float_as_uint(x), false, false);
  x = __uint_as_float(s[0]) + __uint_as_float(s[1]);
  auto t = __builtin_amdgcn_permlane32_swap(__float_as_uint(x), __float_as_uint(x), false, false);
  return __uint_as_float(t[0]) + __uint_as_float(t[1]);
}
__device__ __forceinline__ float xrow_max(float x) {
  auto s = __builtin_amdgcn_permlane16_swap(__float_as_uint(x), __float_as_uint(x), false, false);
  x = fmaxf(__uint_as_float(s[0]), __uint_as_float(s[1]));
  auto t = __builtin_amdgcn_permlane32_swap(__float_as_uint(x), __float_as_uint(x), false, false);
  return fmaxf(__uint_as_float(t[0]), __uint_as_float(t[1]));
}
__device__ __forceinline__ float row16_sum(float x) {
  x += dpp<XOR1>(x); x += dpp<XOR2>(x); x += dpp<ROR4>(x); x += dpp<XOR8>(x); return x;
}
__device__ __forceinline__ float wave_sum(float x) { return xrow_sum(row16_sum(x)); }
__device__ __forceinline__ u32x4 ldnt(const void* p) { return __builtin_nontemporal_load(reinterpret_cast<const u32x4*>(p)); }
__device__ __forceinline__ u32x4 ld16(const void* p) { return *reinterpret_cast<const u32x4*>(p); }

#define STAMP(i) do {} while (0)
constexpr int RING_W = 16384;
constexpr int L_RING = 0;
constexpr int L_XN = NW * RING_W / 4;
constexpr int L_QP = L_XN + 2 * DM;
constexpr int L_MRG = L_QP + NW * DH;
constexpr int L_O = L_MRG + 16;
constexpr int L_FP = L_O + DH;
constexpr int L_HH = L_FP + NW * 128;
constexpr int L_TK = L_HH + 64;
constexpr int L_END = L_TK + 4;

__device__ __forceinline__ float gelu_tanh(float x) {
  const float k0 = 0.7978845608028654f, k1 = 0.044715f;
  float u = k0 * (x + k1 * x * x * x);
  return 0.5f * x * (1.0f + tanhf(u));
}
__device__ __forceinline__ void dma4k(const void* sbase, u32 voff, u32 ldst) {
  u32 keep;
  asm volatile("s_mov_b32 %0, m0\n\ts_mov_b32 m0, %3\n\ts_nop 0\n\t"
               "global_load_lds_dwordx4 %1, %2 nt\n\t"
               "global_load_lds_dwordx4 %1, %2 offset:1024 nt\n\t"
               "global_load_lds_dwordx4 %1, %2 offset:2048 nt\n\t"
               "global_load_lds_dwordx4 %1, %2 offset:3072 nt\n\t"
               "s_mov_b32 m0, %0"
               : "=&s"(keep) : "v"(voff), "s"(sbase), "s"(ldst) : "memory");
}
__device__ __forceinline__ u32x4 ldasm(const void* p) {
  u32x4 r; asm volatile("global_load_dwordx4 %0, %1, off nt" : "=v"(r) : "v"(p) : "memory"); return r;
}
#define WAITV(n) asm volatile("s_waitcnt vmcnt(" #n ")" ::: "memory")
#define WAITL() asm volatile("s_waitcnt lgkmcnt(0)" ::: "memory")
#define PIN4(n, a) asm volatile("s_waitcnt vmcnt(" #n ")" : "+v"((a)[0]), "+v"((a)[1]), "+v"((a)[2]), "+v"((a)[3]) :: "memory")
#define PIN8(n, a) asm volatile("s_waitcnt vmcnt(" #n ")" : "+v"((a)[0]), "+v"((a)[1]), "+v"((a)[2]), "+v"((a)[3]), "+v"((a)[4]), "+v"((a)[5]), "+v"((a)[6]), "+v"((a)[7]) :: "memory")

__global__ void __launch_bounds__(NT, 2) k1_main(const __hip_bfloat16* __restrict__ x, const __hip_bfloat16* __restrict__ kc,
                                                  const __hip_bfloat16* __restrict__ vc, const __hip_bfloat16* __restrict__ wq,
                                                  const __hip_bfloat16* __restrict__ wo, const __hip_bfloat16* __restrict__ wg,
                                                  const __hip_bfloat16* __restrict__ wu, const __hip_bfloat16* __restrict__ wd,
                                                  float* __restrict__ ws) {
  extern __shared__ __attribute__((aligned(16))) float lds[];
  const int tid = threadIdx.x, lane = tid & 63, wave = __builtin_amdgcn_readfirstlane(tid >> 6);
  const int r = lane >> 4, c = lane & 15;
  const int bid = blockIdx.x;
  const int xcd = bid & 7, j = bid >> 3;
  const int h = xcd * 4 + (j >> 3), b = (j >> 2) & 1, sp = j & 3;
  const bool do_ffn = bid < NFS;
  STAMP(0);
  u32x4 xr[2];
  xr[0] = ldasm(x + lane * 8); xr[1] = ldasm(x + DM + lane * 8);
  u32x4 wqr[16];
  {
    const __hip_bfloat16* p = wq + (size_t)(wave * 64 + r * 16) * (NH * DH) + h * DH + c * 8;
#pragma unroll
    for (int i = 0; i < 16; ++i) wqr[i] = ldasm(p + (size_t)i * (NH * DH));
  }
  const size_t kvbase = ((size_t)(b * NH + h) * SK + (size_t)sp * TOK_WG) * DH;
  const char* kp = reinterpret_cast<const char*>(kc + kvbase);
  const char* vp = reinterpret_cast<const char*>(vc + kvbase);
  const u32 voff = lane * 16;
  const u32 ring = (u32)(uintptr_t)(__attribute__((address_space(3))) float*)lds + wave * RING_W;
  if (tid == 0) reinterpret_cast<u32*>(lds)[L_TK] = 4 * NW;

  STAMP(4);
  asm volatile("s_waitcnt vmcnt(16)" : "+v"(xr[0]), "+v"(xr[1]) :: "memory");
  STAMP(5);
  {
#pragma unroll
    for (int bb = 0; bb < 2; ++bb) {
      const u32x4 xv = xr[bb];
      float e[8];
#pragma unroll
      for (int k = 0; k < 4; ++k) { e[2 * k] = bflo(xv[k]); e[2 * k + 1] = bfhi(xv[k]); }
      float s = 0.f;
#pragma unroll
      for (int k = 0; k < 8; ++k) s += e[k];
      const float mean = wave_sum(s) * (1.0f / DM);
      float q2 = 0.f;
#pragma unroll
      for (int k = 0; k < 8; ++k) { const float d = e[k] - mean; q2 = fmaf(d, d, q2); }
      const float var = rbf(wave_sum(q2) * (1.0f / DM));
      const float mb = rbf(mean);
      const float den = rbf(sqrtf(rbf(var + 1e-5f)));
      float o[8];
#pragma unroll
      for (int k = 0; k < 8; ++k) o[k] = rbf(rbf(e[k] - mb) / den);
      if (wave == 0) {
        *reinterpret_cast<f32x4*>(lds + L_XN + bb * DM + lane * 8) = f32x4{o[0], o[1], o[2], o[3]};
        *reinterpret_cast<f32x4*>(lds + L_XN + bb * DM + lane * 8 + 4) = f32x4{o[4], o[5], o[6], o[7]};
      }
    }
  }
  dma4k(kp + (size_t)wave * 4096, voff, ring);
  dma4k(vp + (size_t)wave * 4096, voff, ring + 4096);
  WAITL(); __builtin_amdgcn_s_barrier(); asm volatile("" ::: "memory");
  STAMP(6);
  u32 qpk[4];
  {
    float a[8];
#pragma unroll
    for (int k = 0; k < 8; ++k) a[k] = 0.f;
    const float* xs = lds + L_XN + b * DM + wave * 64 + r * 16;
    f32x4 xv[4];
#pragma unroll
    for (int i4 = 0; i4 < 4; ++i4) xv[i4] = *reinterpret_cast<const f32x4*>(xs + i4 * 4);
    PIN8(16, wqr);
#pragma unroll
    for (int i = 0; i < 8; ++i) {
      const u32x4 w = wqr[i];
#pragma unroll
      for (int k = 0; k < 4; ++k) { a[2 * k] = fmaf(xv[i >> 2][i & 3], bflo(w[k]), a[2 * k]); a[2 * k + 1] = fmaf(xv[i >> 2][i & 3], bfhi(w[k]), a[2 * k + 1]); }
    }
    PIN8(8, wqr + 8);
    STAMP(7);
    dma4k(kp + (size_t)(NW + wave) * 4096, voff, ring + 8192);
    dma4k(vp + (size_t)(NW + wave) * 4096, voff, ring + 12288);
#pragma unroll
    for (int i = 8; i < 16; ++i) {
      const u32x4 w = wqr[i];
#pragma unroll
      for (int k = 0; k < 4; ++k) { a[2 * k] = fmaf(xv[i >> 2][i & 3], bflo(w[k]), a[2 * k]); a[2 * k + 1] = fmaf(xv[i >> 2][i & 3], bfhi(w[k]), a[2 * k + 1]); }
    }
#pragma unroll
    for (int k = 0; k < 8; ++k) a[k] = xrow_sum(a[k]);
    if (r == 0) {
      *reinterpret_cast<f32x4*>(lds + L_QP + wave * DH + c * 8) = f32x4{a[0], a[1], a[2], a[3]};
      *reinterpret_cast<f32x4*>(lds + L_QP + wave * DH + c * 8 + 4) = f32x4{a[4], a[5], a[6], a[7]};
    }
    WAITL(); __builtin_amdgcn_s_barrier(); asm volatile("" ::: "memory");
    f32x4 s0 = {0.f, 0.f, 0.f, 0.f}, s1 = {0.f, 0.f, 0.f, 0.f};
#pragma unroll
    for (int w = 0; w < NW; ++w) {
      s0 += *reinterpret_cast<const f32x4*>(lds + L_QP + w * DH + c * 8);
      s1 += *reinterpret_cast<const f32x4*>(lds + L_QP + w * DH + c * 8 + 4);
    }
    qpk[0] = cvtpk(s0[0], s0[1]); qpk[1] = cvtpk(s0[2], s0[3]); qpk[2] = cvtpk(s1[0], s1[1]); qpk[3] = cvtpk(s1[2], s1[3]);
  }
  STAMP(1);
  u32x4 fg[4], fu[4], fd[4];
  if (do_ffn) {
    const int f0 = bid * FSL;
    const int rr = lane >> 2, cc = lane & 3;
    const size_t off = (size_t)(wave * 64 + rr * 4) * FF + f0 + cc * 8;
#pragma unroll
    for (int i = 0; i < 4; ++i) { fg[i] = ldasm(wg + off + (size_t)i * FF); fu[i] = ldasm(wu + off + (size_t)i * FF); }
#pragma unroll
    for (int i = 0; i < 4; ++i) fd[i] = ldasm(wd + (size_t)(f0 + (lane >> 3) * 4 + i) * DM + wave * 64 + (lane & 7) * 8);
  } else {
#pragma unroll
    for (int i = 0; i < 4; ++i) { fg[i] = u32x4{0, 0, 0, 0}; fu[i] = u32x4{0, 0, 0, 0}; fd[i] = u32x4{0, 0, 0, 0}; }
  }

  constexpr float SCALE = 0.08838834764831845f;
  constexpr float LOG2E = 1.4426950408889634f;
  float m = -INFINITY, l = 0.f, acc[8];
#pragma unroll
  for (int k = 0; k < 8; ++k) acc[k] = 0.f;
  const bool b0 = (c & 1) != 0, b1 = (c & 2) != 0;
  const char* lbase = reinterpret_cast<const char*>(lds) + wave * RING_W + lane * 16;
  float p0, p1, p2, p3;

  auto qk = [&](u32 slot) {
    u32x4 K[4];
#pragma unroll
    for (int t = 0; t < 4; ++t) K[t] = *reinterpret_cast<const u32x4*>(lbase + slot + t * 1024);
    float d[4];
#pragma unroll
    for (int t = 0; t < 4; ++t) {
      float s = dot2(K[t][0], qpk[0], 0.f);
      s = dot2(K[t][1], qpk[1], s); s = dot2(K[t][2], qpk[2], s); s = dot2(K[t][3], qpk[3], s);
      d[t] = s;
    }
    float e0 = b0 ? d[1] : d[0], f0 = b0 ? d[0] : d[1];
    float e1 = b0 ? d[3] : d[2], f1 = b0 ? d[2] : d[3];
    e0 += dpp<XOR1>(f0); e1 += dpp<XOR1>(f1);
    float g0 = b1 ? e1 : e0, g1 = b1 ? e0 : e1;
    g0 += dpp<XOR2>(g1);
    g0 += dpp<ROR4>(g0); g0 += dpp<XOR8>(g0);
    const float sc = rbf(rbf(g0) * SCALE) * LOG2E;
    float tm = fmaxf(sc, dpp<XOR1>(sc)); tm = fmaxf(tm, dpp<XOR2>(tm));
    const float mn = fmaxf(m, tm);
    const float alpha = __builtin_amdgcn_exp2f(m - mn), p = __builtin_amdgcn_exp2f(sc - mn);
    m = mn;
    l = fmaf(l, alpha, p);
    p0 = dpp<0x00>(p); p1 = dpp<0x55>(p); p2 = dpp<0xAA>(p); p3 = dpp<0xFF>(p);
#pragma unroll
    for (int k = 0; k < 8; ++k) acc[k] *= alpha;
  };
  u32* tkp = reinterpret_cast<u32*>(lds) + L_TK;
  auto ticket = [&]() -> u32 { u32 t = 0; if (lane == 0) t = __hip_atomic_fetch_add(tkp, 1u, __ATOMIC_RELAXED, __HIP_MEMORY_SCOPE_WORKGROUP); return t; };

  u32 slot = 0;
  auto stage = [&](u32 t) __attribute__((always_inline)) {
    WAITV(12);
    u32x4 K[4];
#pragma unroll
    for (int i = 0; i < 4; ++i) K[i] = *reinterpret_cast<const u32x4*>(lbase + slot + i * 1024);
    WAITL();
    dma4k(kp + (size_t)t * 4096, voff, ring + slot);
    {
      float d[4];
#pragma unroll
      for (int i = 0; i < 4; ++i) {
        float s = dot2(K[i][0], qpk[0], 0.f);
        s = dot2(K[i][1], qpk[1], s); s = dot2(K[i][2], qpk[2], s); s = dot2(K[i][3], qpk[3], s);
        d[i] = s;
      }
      float e0 = b0 ? d[1] : d[0], f0 = b0 ? d[0] : d[1];
      float e1 = b0 ? d[3] : d[2], f1 = b0 ? d[2] : d[3];
      e0 += dpp<XOR1>(f0); e1 += dpp<XOR1>(f1);
      float g0 = b1 ? e1 : e0, g1 = b1 ? e0 : e1;
      g0 += dpp<XOR2>(g1);
      g0 += dpp<ROR4>(g0); g0 += dpp<XOR8>(g0);
      const float sc = rbf(rbf(g0) * SCALE) * LOG2E;
      float tm = fmaxf(sc, dpp<XOR1>(sc)); tm = fmaxf(tm, dpp<XOR2>(tm));
      const float mn = fmaxf(m, tm);
      const float alpha = __builtin_amdgcn_exp2f(m - mn), p = __builtin_amdgcn_exp2f(sc - mn);
      m = mn;
      l = fmaf(l, alpha, p);
      p0 = dpp<0x00>(p); p1 = dpp<0x55>(p); p2 = dpp<0xAA>(p); p3 = dpp<0xFF>(p);
#pragma unroll
      for (int k = 0; k < 8; ++k) acc[k] *= alpha;
    }
    WAITV(12);
    u32x4 V[4];
#pragma unroll
    for (int i = 0; i < 4; ++i) V[i] = *reinterpret_cast<const u32x4*>(lbase + slot + 4096 + i * 1024);
    WAITL();
    dma4k(vp + (size_t)t * 4096, voff, ring + slot + 4096);
#pragma unroll
    for (int i = 0; i < 4; ++i) {
      const float pt = i == 0 ? p0 : i == 1 ? p1 : i == 2 ? p2 : p3;
#pragma unroll
      for (int k = 0; k < 4; ++k) { acc[2 * k] = fmaf(pt, bflo(V[i][k]), acc[2 * k]); acc[2 * k + 1] = fmaf(pt, bfhi(V[i][k]), acc[2 * k + 1]); }
    }
    slot ^= 8192;
  };
#pragma unroll
  for (int s2 = 0; s2 < 2; ++s2) stage((u32)((2 + s2) * NW + wave));
  u32 tk = ticket();
  if (do_ffn) {
      PIN4(12, fg); PIN4(12, fu); PIN4(12, fd);
      const int rr = lane >> 2;
      float ag[2][8], au[2][8];
#pragma unroll
      for (int bb = 0; bb < 2; ++bb) {
        const f32x4 xv = *reinterpret_cast<const f32x4*>(lds + L_XN + bb * DM + wave * 64 + rr * 4);
#pragma unroll
        for (int k = 0; k < 8; ++k) { ag[bb][k] = 0.f; au[bb][k] = 0.f; }
#pragma unroll
        for (int i = 0; i < 4; ++i)
#pragma unroll
          for (int k = 0; k < 4; ++k) {
            ag[bb][2 * k] = fmaf(xv[i], bflo(fg[i][k]), ag[bb][2 * k]); ag[bb][2 * k + 1] = fmaf(xv[i], bfhi(fg[i][k]), ag[bb][2 * k + 1]);
            au[bb][2 * k] = fmaf(xv[i], bflo(fu[i][k]), au[bb][2 * k]); au[bb][2 * k + 1] = fmaf(xv[i], bfhi(fu[i][k]), au[bb][2 * k + 1]);
          }
#pragma unroll
        for (int k = 0; k < 8; ++k) {
          float t2 = ag[bb][k]; t2 += dpp<ROR4>(t2); t2 += dpp<XOR8>(t2); ag[bb][k] = xrow_sum(t2);
          float u = au[bb][k]; u += dpp<ROR4>(u); u += dpp<XOR8>(u); au[bb][k] = xrow_sum(u);
        }
      }
      if (lane < 4) {
#pragma unroll
        for (int bb = 0; bb < 2; ++bb) {
          float* pg = lds + L_FP + ((wave * 2 + 0) * 2 + bb) * 32 + lane * 8;
          float* pu = lds + L_FP + ((wave * 2 + 1) * 2 + bb) * 32 + lane * 8;
          *reinterpret_cast<f32x4*>(pg) = f32x4{ag[bb][0], ag[bb][1], ag[bb][2], ag[bb][3]};
          *reinterpret_cast<f32x4*>(pg + 4) = f32x4{ag[bb][4], ag[bb][5], ag[bb][6], ag[bb][7]};
          *reinterpret_cast<f32x4*>(pu) = f32x4{au[bb][0], au[bb][1], au[bb][2], au[bb][3]};
          *reinterpret_cast<f32x4*>(pu + 4) = f32x4{au[bb][4], au[bb][5], au[bb][6], au[bb][7]};
        }
      }
      WAITL(); __builtin_amdgcn_s_barrier();
      if (tid < 64) {
        float g = 0.f, u = 0.f;
#pragma unroll
        for (int w = 0; w < NW; ++w) { g += lds[L_FP + (w * 2 + 0) * 64 + tid]; u += lds[L_FP + (w * 2 + 1) * 64 + tid]; }
        const float gl = rbf(gelu_tanh(rbf(g)));
        lds[L_HH + tid] = rbf(gl * rbf(u));
      }
      WAITL(); __builtin_amdgcn_s_barrier(); asm volatile("" ::: "memory");
#pragma unroll
      for (int bb = 0; bb < 2; ++bb) {
        const f32x4 hv = *reinterpret_cast<const f32x4*>(lds + L_HH + bb * 32 + (lane >> 3) * 4);
        float z[8];
#pragma unroll
        for (int k = 0; k < 8; ++k) z[k] = 0.f;
#pragma unroll
        for (int i = 0; i < 4; ++i)
#pragma unroll
          for (int k = 0; k < 4; ++k) { z[2 * k] = fmaf(hv[i], bflo(fd[i][k]), z[2 * k]); z[2 * k + 1] = fmaf(hv[i], bfhi(fd[i][k]), z[2 * k + 1]); }
#pragma unroll
        for (int k = 0; k < 8; ++k) { float t2 = z[k]; t2 += dpp<XOR8>(t2); z[k] = xrow_sum(t2); }
        if (lane < 8) {
          float* pz = ws + WS_Z + (size_t)(bid * 2 + bb) * DM + wave * 64 + lane * 8;
          *reinterpret_cast<f32x4*>(pz) = f32x4{z[0], z[1], z[2], z[3]};
          *reinterpret_cast<f32x4*>(pz + 4) = f32x4{z[4], z[5], z[6], z[7]};
        }
      }
  }
  u32x4 wor[16];
  {
    const __hip_bfloat16* p = wo + (size_t)(h * DH + (lane >> 3) * 16) * DM + wave * 64 + (lane & 7) * 8;
#pragma unroll
    for (int i = 0; i < 16; ++i) wor[i] = ldasm(p + (size_t)i * DM);
  }

  for (;;) {
    const u32 t = __builtin_amdgcn_readfirstlane(tk);
    if (t >= (u32)NTWG) break;
    tk = ticket();
    stage(t);
  }
  auto pv = [&](u32 sl) {
    u32x4 V[4];
#pragma unroll
    for (int i = 0; i < 4; ++i) V[i] = *reinterpret_cast<const u32x4*>(lbase + sl + 4096 + i * 1024);
#pragma unroll
    for (int i = 0; i < 4; ++i) {
      const float pt = i == 0 ? p0 : i == 1 ? p1 : i == 2 ? p2 : p3;
#pragma unroll
      for (int k = 0; k < 4; ++k) { acc[2 * k] = fmaf(pt, bflo(V[i][k]), acc[2 * k]); acc[2 * k + 1] = fmaf(pt, bfhi(V[i][k]), acc[2 * k + 1]); }
    }
  };
  WAITV(12); qk(slot);
  WAITV(8); pv(slot);
  WAITV(4); qk(slot ^ 8192);
  WAITV(0); pv(slot ^ 8192);
  STAMP(2);

  l += dpp<XOR1>(l); l += dpp<XOR2>(l);
  {
    const float mw = xrow_max(m);
    const float sc = __builtin_amdgcn_exp2f(m - mw);
    l = xrow_sum(l * sc);
#pragma unroll
    for (int k = 0; k < 8; ++k) acc[k] = xrow_sum(acc[k] * sc);
    if (r == 0) {
      *reinterpret_cast<f32x4*>(lds + L_QP + wave * DH + c * 8) = f32x4{acc[0], acc[1], acc[2], acc[3]};
      *reinterpret_cast<f32x4*>(lds + L_QP + wave * DH + c * 8 + 4) = f32x4{acc[4], acc[5], acc[6], acc[7]};
      if (c == 0) { lds[L_MRG + wave * 2] = mw; lds[L_MRG + wave * 2 + 1] = l; }
    }
  }
  WAITL(); __builtin_amdgcn_s_barrier(); asm volatile("" ::: "memory");
  float M = -INFINITY, L = 0.f;
  {
#pragma unroll
    for (int w = 0; w < NW; ++w) M = fmaxf(M, lds[L_MRG + w * 2]);
    float od = 0.f;
#pragma unroll
    for (int w = 0; w < NW; ++w) {
      const float wt = __builtin_amdgcn_exp2f(lds[L_MRG + w * 2] - M);
      L = fmaf(wt, lds[L_MRG + w * 2 + 1], L);
      if (tid < DH) od = fmaf(wt, lds[L_QP + w * DH + tid], od);
    }
    if (tid < DH) lds[L_O + tid] = od;
  }
  WAITL(); __builtin_amdgcn_s_barrier(); asm volatile("" ::: "memory");
  {
    f32x4 ov[4];
#pragma unroll
    for (int i = 0; i < 4; ++i) ov[i] = *reinterpret_cast<const f32x4*>(lds + L_O + (lane >> 3) * 16 + i * 4);
    PIN8(0, wor); PIN8(0, wor + 8);
    float y[8];
#pragma unroll
    for (int k = 0; k < 8; ++k) y[k] = 0.f;
#pragma unroll
    for (int i = 0; i < 16; ++i) {
      const float oi = ov[i >> 2][i & 3];
#pragma unroll
      for (int k = 0; k < 4; ++k) { y[2 * k] = fmaf(oi, bflo(wor[i][k]), y[2 * k]); y[2 * k + 1] = fmaf(oi, bfhi(wor[i][k]), y[2 * k + 1]); }
    }
#pragma unroll
    for (int k = 0; k < 8; ++k) { float t2 = y[k]; t2 += dpp<XOR8>(t2); y[k] = xrow_sum(t2); }
    const int slotid = (b * NH + h) * NSP + sp;
    if (lane < 8) {
      float* py = ws + WS_Y + (size_t)slotid * DM + wave * 64 + lane * 8;
      *reinterpret_cast<f32x4*>(py) = f32x4{y[0], y[1], y[2], y[3]};
      *reinterpret_cast<f32x4*>(py + 4) = f32x4{y[4], y[5], y[6], y[7]};
    }
    if (tid == 0) *reinterpret_cast<f32x2*>(ws + WS_ML + slotid * 2) = f32x2{M, L};
  }
  STAMP(3);
}

__global__ void __launch_bounds__(128) k2_final(const __hip_bfloat16* __restrict__ x, const float* __restrict__ ws, __hip_bfloat16* __restrict__ out) {
  __shared__ float red[2][2][16];
  const int tid = threadIdx.x, lane = tid & 63, wave = tid >> 6;
  const int b = blockIdx.x >> 5, cg = blockIdx.x & 31;
  const int slot = b * NH * NSP + tid;
  const f32x4* py = reinterpret_cast<const f32x4*>(ws + WS_Y + (size_t)slot * DM + cg * 16);
  f32x4 y[4], z[4];
#pragma unroll
  for (int i = 0; i < 4; ++i) y[i] = py[i];
  const f32x2 ml = *reinterpret_cast<const f32x2*>(ws + WS_ML + slot * 2);
  if (tid < NFS) {
    const f32x4* pz = reinterpret_cast<const f32x4*>(ws + WS_Z + (size_t)(tid * 2 + b) * DM + cg * 16);
#pragma unroll
    for (int i = 0; i < 4; ++i) z[i] = pz[i];
  } else {
#pragma unroll
    for (int i = 0; i < 4; ++i) z[i] = f32x4{0.f, 0.f, 0.f, 0.f};
  }
  float M = fmaxf(ml[0], dpp<XOR1>(ml[0])); M = fmaxf(M, dpp<XOR2>(M));
  const float e = __builtin_amdgcn_exp2f(ml[0] - M);
  float L = e * ml[1]; L += dpp<XOR1>(L); L += dpp<XOR2>(L);
  const float wt = e / L;
  float sy[16], sz[16];
#pragma unroll
  for (int i = 0; i < 4; ++i)
#pragma unroll
    for (int k = 0; k < 4; ++k) { sy[i * 4 + k] = wave_sum(y[i][k] * wt); sz[i * 4 + k] = wave_sum(z[i][k]); }
  if (lane == 0) {
#pragma unroll
    for (int k = 0; k < 16; ++k) { red[wave][0][k] = sy[k]; red[wave][1][k] = sz[k]; }
  }
  __syncthreads();
  if (tid < 16) {
    const float s = rbf(red[0][0][tid] + red[1][0][tid]);
    const float f = rbf(red[0][1][tid] + red[1][1][tid]);
    const float g = rbf(f / (1.0f + __expf(-f)));
    const float xv = __bfloat162float(x[b * DM + cg * 16 + tid]);
    out[b * DM + cg * 16 + tid] = __float2bfloat16(xv + rbf(s * g));
  }
}

extern "C" void kernel_launch(void* const* d_in, const int* in_sizes, int n_in, void* d_out, int out_size, void* d_ws, size_t ws_size,
                              hipStream_t stream) {
  const __hip_bfloat16* x = (const __hip_bfloat16*)d_in[0];
  const __hip_bfloat16* kc = (const __hip_bfloat16*)d_in[1];
  const __hip_bfloat16* vc = (const __hip_bfloat16*)d_in[2];
  const __hip_bfloat16* wq = (const __hip_bfloat16*)d_in[3];
  const __hip_bfloat16* wo = (const __hip_bfloat16*)d_in[4];
  const __hip_bfloat16* wg = (const __hip_bfloat16*)d_in[6];
  const __hip_bfloat16* wu = (const __hip_bfloat16*)d_in[7];
  const __hip_bfloat16* wd = (const __hip_bfloat16*)d_in[8];
  float* ws = (float*)d_ws;
  hipFuncSetAttribute((const void*)k1_main, hipFuncAttributeMaxDynamicSharedMemorySize, L_END * 4);
  k1_main<<<NWG, NT, L_END * 4, stream>>>(x, kc, vc, wq, wo, wg, wu, wd, ws);
  k2_final<<<NB * 32, 128, 0, stream>>>(x, ws, (__hip_bfloat16*)d_out);
}
```

```cpp
#include <hip/hip_runtime.h>
#include <hip/hip_bf16.h>
#include <stdint.h>

typedef unsigned int u32;
typedef __attribute__((ext_vector_type(4))) unsigned int u32x4;
typedef __attribute__((ext_vector_type(4))) float f32x4;
typedef __attribute__((ext_vector_type(2))) float f32x2;
typedef __attribute__((ext_vector_type(2))) __bf16 bf16x2;

constexpr int NB = 2, DM = 512, NH = 32, SK = 16384, DH = 128, FF = 1376;
constexpr int NSP = 4;
constexpr int NW = 8;
constexpr int NT = (NW + 1) * 64;
constexpr int TOK_WG = SK / NSP;
constexpr int TILE = 16;
constexpr int NTILE = TOK_WG / (TILE * NW);
constexpr int NTWG = NTILE * NW;
constexpr int FSL = 32;
constexpr int NFS = FF / FSL;
constexpr int NWG = NB * NH * NSP;

__device__ __forceinline__ float bflo(u32 v) { return __builtin_bit_cast(float, v << 16); }
__device__ __forceinline__ float bfhi(u32 v) { return __builtin_bit_cast(float, v & 0xffff0000u); }
__device__ __forceinline__ u32 cvtpk(float lo, float hi) {
  u32 r; asm("v_cvt_pk_bf16_f32 %0, %1, %2" : "=v"(r) : "v"(lo), "v"(hi)); return r;
}
__device__ __forceinline__ float rbf(float x) { return bflo(cvtpk(x, x)); }
__device__ __forceinline__ float dot2(u32 k, u32 q, float acc) {
  return __builtin_amdgcn_fdot2_f32_bf16(__builtin_bit_cast(bf16x2, k), __builtin_bit_cast(bf16x2, q), acc, false);
}
template <int CTRL> __device__ __forceinline__ float dpp(float x) {
  return __builtin_bit_cast(float, __builtin_amdgcn_mov_dpp(__builtin_bit_cast(int, x), CTRL, 0xf, 0xf, true));
}
constexpr int XOR1 = 0xB1, XOR2 = 0x4E, XOR7 = 0x141, XOR8 = 0x128, ROR4 = 0x124;
__device__ __forceinline__ float xrow_sum(float x) {
  auto s = __builtin_amdgcn_permlane16_swap(__float_as_uint(x), __float_as_uint(x), false, false);
  x = __uint_as_float(s[0]) + __uint_as_float(s[1]);
  auto t = __builtin_amdgcn_permlane32_swap(__float_as_uint(x), __float_as_uint(x), false, false);
  return __uint_as_float(t[0]) + __uint_as_float(t[1]);
}
__device__ __forceinline__ float xrow_max(float x) {
  auto s = __builtin_amdgcn_permlane16_swap(__float_as_uint(x), __float_as_uint(x), false, false);
  x = fmaxf(__uint_as_float(s[0]), __uint_as_float(s[1]));
  auto t = __builtin_amdgcn_permlane32_swap(__float_as_uint(x), __float_as_uint(x), false, false);
  return fmaxf(__uint_as_float(t[0]), __uint_as_float(t[1]));
}
__device__ __forceinline__ float row16_sum(float x) {
  x += dpp<XOR1>(x); x += dpp<XOR2>(x); x += dpp<ROR4>(x); x += dpp<XOR8>(x); return x;
}
__device__ __forceinline__ float wave_sum(float x) { return xrow_sum(row16_sum(x)); }
__device__ __forceinline__ u32x4 ldnt(const void* p) { return __builtin_nontemporal_load(reinterpret_cast<const u32x4*>(p)); }
__device__ __forceinline__ u32x4 ld16(const void* p) { return *reinterpret_cast<const u32x4*>(p); }

typedef unsigned long long u64;
constexpr u32 W_QD = 0;
constexpr u32 W_O1 = 2u << 20;
constexpr u32 W_O2 = W_O1 + (256 << 10);
constexpr u32 W_Z = W_O1 + (512 << 10);
constexpr u32 W_CH = 3u << 20;
constexpr u32 W_R = W_CH + (32 << 10);
constexpr u32 W_LCG = W_R + (4 << 10);
constexpr u32 W_END = W_LCG + (4 << 10);
constexpr u32 SENT = 0xffffffffu;
constexpr int LEADER = 255;

typedef __attribute__((ext_vector_type(2))) unsigned int u32x2;
typedef __amdgpu_buffer_rsrc_t rsrc_t;
#define MKRS(p, bytes) __builtin_amdgcn_make_buffer_rsrc((void*)(p), 0, (int)(bytes), 0x00020000)
__device__ __forceinline__ u32x4 cl4(rsrc_t rs, u32 v, u32 so) { return __builtin_amdgcn_raw_buffer_load_b128(rs, (int)v, (int)so, 16); }
__device__ __forceinline__ u32x2 cl2(rsrc_t rs, u32 v, u32 so) { return __builtin_amdgcn_raw_buffer_load_b64(rs, (int)v, (int)so, 16); }
__device__ __forceinline__ u32 cl1(rsrc_t rs, u32 v, u32 so) { return __builtin_amdgcn_raw_buffer_load_b32(rs, (int)v, (int)so, 16); }
__device__ __forceinline__ void cs4(rsrc_t rs, u32 v, u32 so, u32x4 d) { __builtin_amdgcn_raw_buffer_store_b128(d, rs, (int)v, (int)so, 16); }
__device__ __forceinline__ void cs2(rsrc_t rs, u32 v, u32 so, u32x2 d) { __builtin_amdgcn_raw_buffer_store_b64(d, rs, (int)v, (int)so, 16); }
__device__ __forceinline__ void cs1(rsrc_t rs, u32 v, u32 so, u32 d) { __builtin_amdgcn_raw_buffer_store_b32(d, rs, (int)v, (int)so, 16); }
__device__ __forceinline__ u32x4 wl4(rsrc_t rs, u32 v, u32 so) { return __builtin_amdgcn_raw_buffer_load_b128(rs, (int)v, (int)so, 2); }
__device__ __forceinline__ u32 wl1(rsrc_t rs, u32 v, u32 so) { return __builtin_amdgcn_raw_buffer_load_b32(rs, (int)v, (int)so, 2); }
#define FENCE() asm volatile("" ::: "memory")
#define POLL() asm volatile("" ::: "memory")
__device__ __forceinline__ u32 canon(float f) { const u32 v = __float_as_uint(f); return v == SENT ? 0x7fffffffu : v; }
__device__ __forceinline__ float asf(u32 v) { return __uint_as_float(v); }
__device__ __forceinline__ bool valid4(u32x4 v) { return (v[0] != SENT) & (v[1] != SENT) & (v[2] != SENT) & (v[3] != SENT); }
__device__ __forceinline__ int wgid(int h, int j) { return (h >> 2) + 8 * (((h & 3) << 3) + j); }

#define STAMP(i) do {} while (0)
#define THR 1
constexpr int RING_W = 16384;
constexpr int L_XN = NW * RING_W / 4;
constexpr int L_QP = L_XN + 2 * DM;
constexpr int L_MRG = L_QP + NW * DH;
constexpr int L_QK = L_MRG + 16;
constexpr int L_OP = L_QK + 64;
constexpr int L_FP = L_OP + 128;
constexpr int L_HH = L_FP + 128;
constexpr int L_TK = L_HH + 64;
constexpr int L_END = L_TK + 4;

__device__ __forceinline__ float gelu_tanh(float x) {
  const float k0 = 0.7978845608028654f, k1 = 0.044715f;
  float u = k0 * (x + k1 * x * x * x);
  return 0.5f * x * (1.0f + tanhf(u));
}
__device__ __forceinline__ void dma4k(const void* sbase, u32 voff, u32 ldst) {
  u32 keep;
  asm volatile("s_mov_b32 %0, m0\n\ts_mov_b32 m0, %3\n\ts_nop 0\n\t"
               "global_load_lds_dwordx4 %1, %2 nt\n\t"
               "global_load_lds_dwordx4 %1, %2 offset:1024 nt\n\t"
               "global_load_lds_dwordx4 %1, %2 offset:2048 nt\n\t"
               "global_load_lds_dwordx4 %1, %2 offset:3072 nt\n\t"
               "s_mov_b32 m0, %0"
               : "=&s"(keep) : "v"(voff), "s"(sbase), "s"(ldst) : "memory");
}
__device__ __forceinline__ void ld4k(u32x4* R, const void* sbase, u32 voff) {
  asm volatile("global_load_dwordx4 %0, %4, %5 nt\n\t"
               "global_load_dwordx4 %1, %4, %5 offset:1024 nt\n\t"
               "global_load_dwordx4 %2, %4, %5 offset:2048 nt\n\t"
               "global_load_dwordx4 %3, %4, %5 offset:3072 nt"
               : "=&v"(R[0]), "=&v"(R[1]), "=&v"(R[2]), "=&v"(R[3]) : "v"(voff), "s"(sbase) : "memory");
}
#define PIN4(n, a) asm volatile("s_waitcnt vmcnt(" #n ")" : "+v"((a)[0]), "+v"((a)[1]), "+v"((a)[2]), "+v"((a)[3]) :: "memory")
#define WAITV(n) asm volatile("s_waitcnt vmcnt(" #n ")" ::: "memory")
#define DEPTH 12
#define WAITV2(n) WAITV(n)
#define WAITD() WAITV2(DEPTH)
#define WAITL() asm volatile("s_waitcnt lgkmcnt(0)" ::: "memory")
#define BAR() do { WAITL(); __builtin_amdgcn_s_barrier(); asm volatile("" ::: "memory"); } while (0)

__global__ void __launch_bounds__(NT) fused_k(const __hip_bfloat16* __restrict__ x, const __hip_bfloat16* __restrict__ kc,
                                               const __hip_bfloat16* __restrict__ vc, const __hip_bfloat16* __restrict__ wq,
                                               const __hip_bfloat16* __restrict__ wo, const __hip_bfloat16* __restrict__ wg,
                                               const __hip_bfloat16* __restrict__ wu, const __hip_bfloat16* __restrict__ wd,
                                               char* ws, __hip_bfloat16* __restrict__ out) {
  extern __shared__ __attribute__((aligned(16))) float lds[];
  const int tid = threadIdx.x, lane = tid & 63, wave = __builtin_amdgcn_readfirstlane(tid >> 6);
  const int r = lane >> 4, c = lane & 15;
  const int bid = blockIdx.x;
  const int xcd = bid & 7, j = (bid >> 3) & 7, hl = bid >> 6;
  const int h = xcd * 4 + hl, b = j >> 2, sp = j & 3;
  const int fe = (hl * 2 + b) * 8 + xcd;
  const bool fgrp = fe < NFS;
  constexpr float LOG2E = 1.4426950408889634f;

  if (wave == 0) {
    __builtin_amdgcn_s_setprio(3);
    STAMP(0);
    const rsrc_t rw = MKRS(ws, W_END);
    const u32 l16 = lane * 16, l8 = lane * 8;
    int spin = 0; (void)spin;
    const u32 inbox = W_QD + bid * 8192;
    const u32 T0 = (u32)__builtin_amdgcn_s_memrealtime();
    const u32 chdst = wgid(h, lane & 7) * 128;
    if (lane < 8 && lane != j) cs1(rw, chdst, W_CH + j * 4, T0);
    const u32x4 xr0 = ld16(x + lane * 8), xr1 = ld16(x + DM + lane * 8);
    u32x4 wqr[16];
    {
      const rsrc_t rq = MKRS(wq, DM * NH * DH * 2);
      const u32 vo = (r * 16 * (NH * DH) + c * 8) * 2;
#pragma unroll
      for (int i = 0; i < 16; ++i) wqr[i] = wl4(rq, vo, ((j * 64 + i) * (NH * DH) + h * DH) * 2);
    }
    u32x4 g[8];
#pragma unroll
    for (int jp = 0; jp < 8; ++jp) g[jp] = cl4(rw, l16, inbox + jp * 1024);
    const u32 g0v = cl1(rw, 0, W_LCG + bid * 8 + 4);
    u32 le[4] = {0, 0, 0, 0};
    if (bid == LEADER) {
#pragma unroll
      for (int k = 0; k < 4; ++k) le[k] = cl1(rw, l8, W_R + 512 * k);
    }
    FENCE();
    __builtin_amdgcn_s_barrier();
    FENCE();
    STAMP(4);
#pragma unroll
    for (int bb = 0; bb < 2; ++bb) {
      const u32x4 xv = bb ? xr1 : xr0;
      float e[8];
#pragma unroll
      for (int k = 0; k < 4; ++k) { e[2 * k] = bflo(xv[k]); e[2 * k + 1] = bfhi(xv[k]); }
      float s = 0.f;
#pragma unroll
      for (int k = 0; k < 8; ++k) s += e[k];
      const float mean = wave_sum(s) * (1.0f / DM);
      float q2 = 0.f;
#pragma unroll
      for (int k = 0; k < 8; ++k) { const float d = e[k] - mean; q2 = fmaf(d, d, q2); }
      const float var = rbf(wave_sum(q2) * (1.0f / DM));
      const float mb = rbf(mean);
      const float den = rbf(sqrtf(rbf(var + 1e-5f)));
      float o[8];
#pragma unroll
      for (int k = 0; k < 8; ++k) o[k] = rbf(rbf(e[k] - mb) / den);
      *reinterpret_cast<f32x4*>(lds + L_XN + bb * DM + lane * 8) = f32x4{o[0], o[1], o[2], o[3]};
      *reinterpret_cast<f32x4*>(lds + L_XN + bb * DM + lane * 8 + 4) = f32x4{o[4], o[5], o[6], o[7]};
    }
    FENCE();
    const u32 chv0 = cl1(rw, (lane & 7) * 4, W_CH + bid * 128);
    FENCE();
    float v[2][2];
    {
      float a[2][8];
#pragma unroll
      for (int bb = 0; bb < 2; ++bb)
#pragma unroll
        for (int k = 0; k < 8; ++k) a[bb][k] = 0.f;
#pragma unroll
      for (int i4 = 0; i4 < 4; ++i4) {
        const f32x4 xa = *reinterpret_cast<const f32x4*>(lds + L_XN + j * 64 + r * 16 + i4 * 4);
        const f32x4 xb = *reinterpret_cast<const f32x4*>(lds + L_XN + DM + j * 64 + r * 16 + i4 * 4);
#pragma unroll
        for (int i = 0; i < 4; ++i)
#pragma unroll
          for (int k = 0; k < 4; ++k) {
            const float wl = bflo(wqr[i4 * 4 + i][k]), wh = bfhi(wqr[i4 * 4 + i][k]);
            a[0][2 * k] = fmaf(xa[i], wl, a[0][2 * k]); a[0][2 * k + 1] = fmaf(xa[i], wh, a[0][2 * k + 1]);
            a[1][2 * k] = fmaf(xb[i], wl, a[1][2 * k]); a[1][2 * k + 1] = fmaf(xb[i], wh, a[1][2 * k + 1]);
          }
      }
#pragma unroll
      for (int bb = 0; bb < 2; ++bb) {
        float t[4];
#pragma unroll
        for (int k = 0; k < 4; ++k) {
          auto sw = __builtin_amdgcn_permlane32_swap(__float_as_uint(a[bb][k]), __float_as_uint(a[bb][4 + k]), false, false);
          t[k] = asf(sw[0]) + asf(sw[1]);
        }
#pragma unroll
        for (int e = 0; e < 2; ++e) {
          auto sw = __builtin_amdgcn_permlane16_swap(__float_as_uint(t[e]), __float_as_uint(t[2 + e]), false, false);
          v[bb][e] = asf(sw[0]) + asf(sw[1]);
        }
      }
    }
    STAMP(5);
    u32 T = T0;
    for (;;) {
      bool hit = false;
#pragma unroll
      for (int jp = 0; jp < 8; ++jp) hit |= (g[jp][1] == T) | (g[jp][3] == T);
      if (__builtin_amdgcn_ballot_w64(hit) == 0) break;
      ++T;
    }
    if (T != T0 && lane < 8 && lane != j) cs1(rw, chdst, W_CH + j * 4, T);
    const u32 gch = __builtin_amdgcn_readfirstlane(g0v) + 1u;
    u32 last[8];
#pragma unroll
    for (int k = 0; k < 8; ++k) last[k] = 0;
    bool first = true;
    const u32 chv_off = (lane & 7) * 4, chs_off = W_CH + bid * 128;
    auto service = [&](u32 chv) __attribute__((always_inline)) {
#pragma unroll
      for (int jc = 0; jc < 8; ++jc) {
        const u32 t = __builtin_amdgcn_readlane(chv, jc);
        if (jc != j && (first || t != last[jc])) {
          last[jc] = t;
          cs4(rw, l16, W_QD + (wgid(h, jc) * 8 + j) * 1024, u32x4{__float_as_uint(v[jc >> 2][0]), t, __float_as_uint(v[jc >> 2][1]), t});
        }
      }
      first = false;
    };
    service(chv0);
    for (;;) {
      POLL();
      const u32 chv = cl1(rw, chv_off, chs_off);
#pragma unroll
      for (int jp = 0; jp < 8; ++jp) g[jp] = cl4(rw, l16, inbox + jp * 1024);
      service(chv);
      bool bad = false;
#pragma unroll
      for (int jp = 0; jp < 8; ++jp) bad |= (jp != j) & ((g[jp][1] != T) | (g[jp][3] != T));
      if (__builtin_amdgcn_ballot_w64(bad) == 0) break;
    }
    {
      const float own0 = b ? v[1][0] : v[0][0], own1 = b ? v[1][1] : v[0][1];
      float q0 = 0.f, q1 = 0.f;
#pragma unroll
      for (int jp = 0; jp < 8; ++jp) { q0 += (jp == j) ? own0 : asf(g[jp][0]); q1 += (jp == j) ? own1 : asf(g[jp][2]); }
      reinterpret_cast<u32*>(lds)[L_QK + 4 * c + r] = cvtpk(q0, q1);
      if (lane == 0) reinterpret_cast<u32*>(lds)[L_TK] = 6 * NW;
    }
    BAR();
    STAMP(1);
    __builtin_amdgcn_s_setprio(0);
    cs4(rw, l16, W_O1 + bid * 1024, u32x4{SENT, SENT, SENT, SENT});
    cs2(rw, l8, W_O2 + bid * 512, u32x2{SENT, SENT});
    if (fgrp && sp == 0) {
#pragma unroll
      for (int k = 0; k < 4; ++k) cs4(rw, l16, W_Z + fe * (2 * DM * 4) + k * 1024, u32x4{SENT, SENT, SENT, SENT});
    }
    if (bid == LEADER) {
#pragma unroll
      for (int k = 0; k < 4; ++k) { le[k] += 1u; cs1(rw, l8, W_LCG + 512 * k, le[k]); }
    }
    WAITV(0);
    {
      u32 lastlc = 0; bool f1 = true, ldone = false;
      for (;;) {
        POLL();
        const u32x2 lcg = cl2(rw, 0, W_LCG + bid * 8);
        const u32 chv = cl1(rw, chv_off, chs_off);
        u32x2 rr4[4];
        if (bid == LEADER) {
#pragma unroll
          for (int k = 0; k < 4; ++k) rr4[k] = cl2(rw, l8, W_R + 512 * k);
        }
        const u32 lc = __builtin_amdgcn_readfirstlane(lcg[0]), lg = __builtin_amdgcn_readfirstlane(lcg[1]);
        if (lg == gch) break;
        if (f1 || lc != lastlc) { if (lane == 0) cs2(rw, 0, W_R + bid * 8, u32x2{lc, gch}); lastlc = lc; f1 = false; }
        service(chv);
        if (bid == LEADER && !ldone) {
          bool bad = false;
#pragma unroll
          for (int k = 0; k < 4; ++k) bad |= rr4[k][0] != le[k];
          if (__builtin_amdgcn_ballot_w64(bad) == 0) {
#pragma unroll
            for (int k = 0; k < 4; ++k) cs1(rw, l8, W_LCG + 512 * k + 4, rr4[k][1]);
            ldone = true;
          }
        }
        __builtin_amdgcn_s_sleep(16);
      }
    }
    STAMP(6);
    if (fgrp && sp == 0) {
      const int f0 = fe * FSL, rr = lane >> 2, cc = lane & 3;
      float ag[2][8], au[2][8];
#pragma unroll
      for (int bb = 0; bb < 2; ++bb)
#pragma unroll
        for (int k = 0; k < 8; ++k) { ag[bb][k] = 0.f; au[bb][k] = 0.f; }
      const rsrc_t rg = MKRS(wg, DM * FF * 2), ru = MKRS(wu, DM * FF * 2), rd = MKRS(wd, DM * FF * 2);
      const u32 vo = (rr * 32 * FF + cc * 8) * 2;
#pragma unroll 1
      for (int ch = 0; ch < 4; ++ch) {
        u32x4 fg[8], fu[8];
#pragma unroll
        for (int i = 0; i < 8; ++i) { const u32 so = ((ch * 8 + i) * FF + f0) * 2; fg[i] = wl4(rg, vo, so); fu[i] = wl4(ru, vo, so); }
#pragma unroll
        for (int i4 = 0; i4 < 2; ++i4) {
          const f32x4 xa = *reinterpret_cast<const f32x4*>(lds + L_XN + rr * 32 + ch * 8 + i4 * 4);
          const f32x4 xb = *reinterpret_cast<const f32x4*>(lds + L_XN + DM + rr * 32 + ch * 8 + i4 * 4);
#pragma unroll
          for (int i = 0; i < 4; ++i)
#pragma unroll
            for (int k = 0; k < 4; ++k) {
              const float gl = bflo(fg[i4 * 4 + i][k]), gh = bfhi(fg[i4 * 4 + i][k]), ul = bflo(fu[i4 * 4 + i][k]), uh = bfhi(fu[i4 * 4 + i][k]);
              ag[0][2 * k] = fmaf(xa[i], gl, ag[0][2 * k]); ag[0][2 * k + 1] = fmaf(xa[i], gh, ag[0][2 * k + 1]);
              ag[1][2 * k] = fmaf(xb[i], gl, ag[1][2 * k]); ag[1][2 * k + 1] = fmaf(xb[i], gh, ag[1][2 * k + 1]);
              au[0][2 * k] = fmaf(xa[i], ul, au[0][2 * k]); au[0][2 * k + 1] = fmaf(xa[i], uh, au[0][2 * k + 1]);
              au[1][2 * k] = fmaf(xb[i], ul, au[1][2 * k]); au[1][2 * k + 1] = fmaf(xb[i], uh, au[1][2 * k + 1]);
            }
        }
      }
#pragma unroll
      for (int bb = 0; bb < 2; ++bb) {
#pragma unroll
        for (int k = 0; k < 8; ++k) {
          float t2 = ag[bb][k]; t2 += dpp<ROR4>(t2); t2 += dpp<XOR8>(t2); ag[bb][k] = xrow_sum(t2);
          float u = au[bb][k]; u += dpp<ROR4>(u); u += dpp<XOR8>(u); au[bb][k] = xrow_sum(u);
        }
        if (lane < 4) {
          float* pg = lds + L_FP + bb * 32 + lane * 8;
          float* pu = lds + L_FP + 64 + bb * 32 + lane * 8;
          *reinterpret_cast<f32x4*>(pg) = f32x4{ag[bb][0], ag[bb][1], ag[bb][2], ag[bb][3]};
          *reinterpret_cast<f32x4*>(pg + 4) = f32x4{ag[bb][4], ag[bb][5], ag[bb][6], ag[bb][7]};
          *reinterpret_cast<f32x4*>(pu) = f32x4{au[bb][0], au[bb][1], au[bb][2], au[bb][3]};
          *reinterpret_cast<f32x4*>(pu + 4) = f32x4{au[bb][4], au[bb][5], au[bb][6], au[bb][7]};
        }
      }
      WAITL();
      {
        const float gl = rbf(gelu_tanh(rbf(lds[L_FP + lane])));
        lds[L_HH + lane] = rbf(gl * rbf(lds[L_FP + 64 + lane]));
      }
      WAITL();
      float z[2][8];
#pragma unroll
      for (int bb = 0; bb < 2; ++bb)
#pragma unroll
        for (int k = 0; k < 8; ++k) z[bb][k] = 0.f;
#pragma unroll 1
      for (int ch = 0; ch < 2; ++ch) {
        u32x4 fd[16];
#pragma unroll
        for (int i = 0; i < 16; ++i) fd[i] = wl4(rd, l16, (f0 + ch * 16 + i) * (DM * 2));
#pragma unroll
        for (int i4 = 0; i4 < 4; ++i4) {
          const f32x4 ha = *reinterpret_cast<const f32x4*>(lds + L_HH + ch * 16 + i4 * 4);
          const f32x4 hb = *reinterpret_cast<const f32x4*>(lds + L_HH + 32 + ch * 16 + i4 * 4);
#pragma unroll
          for (int i = 0; i < 4; ++i)
#pragma unroll
            for (int k = 0; k < 4; ++k) {
              const float wl = bflo(fd[i4 * 4 + i][k]), wh = bfhi(fd[i4 * 4 + i][k]);
              z[0][2 * k] = fmaf(ha[i], wl, z[0][2 * k]); z[0][2 * k + 1] = fmaf(ha[i], wh, z[0][2 * k + 1]);
              z[1][2 * k] = fmaf(hb[i], wl, z[1][2 * k]); z[1][2 * k + 1] = fmaf(hb[i], wh, z[1][2 * k + 1]);
            }
        }
      }
#pragma unroll
      for (int bb = 0; bb < 2; ++bb)
#pragma unroll
        for (int k = 0; k < 2; ++k)
          cs4(rw, lane * 32, W_Z + (fe * 2 + bb) * (DM * 4) + k * 16,
              u32x4{canon(z[bb][4 * k]), canon(z[bb][4 * k + 1]), canon(z[bb][4 * k + 2]), canon(z[bb][4 * k + 3])});
    }
    u32 P0[32], P1[32];
    {
      const rsrc_t ro = MKRS(wo, NH * DH * DM * 2);
      const u32 vo = ((lane >> 5) * 64 * DM + (lane & 31) * 2) * 2, so = (h * DH * DM + j * 64) * 2;
#pragma unroll
      for (int k = 0; k < 32; ++k) { P0[k] = wl1(ro, vo, so + (2 * k) * (DM * 2)); P1[k] = wl1(ro, vo, so + (2 * k + 1) * (DM * 2)); }
#pragma unroll
      for (int k = 0; k < 32; ++k) {
        const u32 A = P0[k], B = P1[k];
        P0[k] = __builtin_amdgcn_perm(B, A, 0x05040100u); P1[k] = __builtin_amdgcn_perm(B, A, 0x07060302u);
        asm volatile("" : "+v"(P0[k]), "+v"(P1[k]));
      }
    }
    const int oidx = (h >> 4) * DM + j * 64 + (h & 15) * 4;
    float gt[4], xo[4];
    {
      u32x4 zv = {0, 0, 0, 0};
      for (;;) {
        POLL();
        if (lane < NFS) zv = cl4(rw, lane * (2 * DM * 4), W_Z + oidx * 4);
        if (__builtin_amdgcn_ballot_w64(!valid4(zv)) == 0) break;
        __builtin_amdgcn_s_sleep(32);
      }
      const u32x2 xv = *reinterpret_cast<const u32x2*>(x + oidx);
#pragma unroll
      for (int k = 0; k < 4; ++k) { const float f = rbf(wave_sum(asf(zv[k]))); gt[k] = rbf(f / (1.0f + __expf(-f))); }
      xo[0] = bflo(xv[0]); xo[1] = bfhi(xv[0]); xo[2] = bflo(xv[1]); xo[3] = bfhi(xv[1]);
    }
    __builtin_amdgcn_s_setprio(3);
    BAR();
    float oM, oL, o0 = 0.f, o1 = 0.f;
    {
      f32x4 ml[4];
#pragma unroll
      for (int i = 0; i < 4; ++i) ml[i] = *reinterpret_cast<const f32x4*>(lds + L_MRG + i * 4);
      oM = fmaxf(fmaxf(fmaxf(ml[0][0], ml[0][2]), fmaxf(ml[1][0], ml[1][2])), fmaxf(fmaxf(ml[2][0], ml[2][2]), fmaxf(ml[3][0], ml[3][2])));
      oL = 0.f;
#pragma unroll
      for (int w = 0; w < NW; ++w) {
        const float wt = __builtin_amdgcn_exp2f(ml[w >> 1][(w & 1) * 2] - oM);
        oL = fmaf(wt, ml[w >> 1][(w & 1) * 2 + 1], oL);
        const f32x2 pv = *reinterpret_cast<const f32x2*>(lds + L_QP + w * DH + lane * 2);
        o0 = fmaf(wt, pv[0], o0); o1 = fmaf(wt, pv[1], o1);
      }
    }
    cs4(rw, l16, W_O1 + bid * 1024, u32x4{canon(o0), canon(o1), canon(oM), canon(oL)});
    STAMP(2);
    for (;;) {
      POLL();
      bool bad = false;
#pragma unroll
      for (int jp = 0; jp < 8; ++jp) g[jp] = cl4(rw, l16, W_O1 + wgid(h, jp) * 1024);
#pragma unroll
      for (int jp = 0; jp < 8; ++jp) bad |= (jp != j) & !valid4(g[jp]);
      if (__builtin_amdgcn_ballot_w64(bad) == 0) break;
    }
    STAMP(7);
#pragma unroll
    for (int bb = 0; bb < 2; ++bb) {
      float pm[4], pl[4], p0[4], p1[4];
#pragma unroll
      for (int s = 0; s < 4; ++s) {
        const int jp = bb * 4 + s; const bool own = jp == j;
        p0[s] = own ? o0 : asf(g[jp][0]); p1[s] = own ? o1 : asf(g[jp][1]); pm[s] = own ? oM : asf(g[jp][2]); pl[s] = own ? oL : asf(g[jp][3]);
      }
      const float Mb = fmaxf(fmaxf(pm[0], pm[1]), fmaxf(pm[2], pm[3]));
      float Lb = 0.f, a0 = 0.f, a1 = 0.f;
#pragma unroll
      for (int s = 0; s < 4; ++s) { const float wt = __builtin_amdgcn_exp2f(pm[s] - Mb); Lb = fmaf(wt, pl[s], Lb); a0 = fmaf(wt, p0[s], a0); a1 = fmaf(wt, p1[s], a1); }
      const float inv = 1.0f / Lb;
      reinterpret_cast<u32*>(lds)[L_OP + bb * 64 + lane] = cvtpk(a0 * inv, a1 * inv);
    }
    WAITL();
    float y[2][2] = {{0.f, 0.f}, {0.f, 0.f}};
#pragma unroll
    for (int k4 = 0; k4 < 8; ++k4)
#pragma unroll
      for (int bb = 0; bb < 2; ++bb) {
        const u32x4 ov = *reinterpret_cast<const u32x4*>(lds + L_OP + bb * 64 + (lane >> 5) * 32 + k4 * 4);
#pragma unroll
        for (int k = 0; k < 4; ++k) { y[bb][0] = dot2(P0[k4 * 4 + k], ov[k], y[bb][0]); y[bb][1] = dot2(P1[k4 * 4 + k], ov[k], y[bb][1]); }
      }
    {
      auto s0 = __builtin_amdgcn_permlane32_swap(__float_as_uint(y[0][0]), __float_as_uint(y[1][0]), false, false);
      auto s1 = __builtin_amdgcn_permlane32_swap(__float_as_uint(y[0][1]), __float_as_uint(y[1][1]), false, false);
      cs2(rw, l8, W_O2 + bid * 512, u32x2{canon(__uint_as_float(s0[0]) + __uint_as_float(s0[1])), canon(__uint_as_float(s1[0]) + __uint_as_float(s1[1]))});
    }
    u32x4 yv;
    {
      const u32 vo = wgid(lane & 31, j) * 512;
      for (;;) {
        POLL();
        yv = cl4(rw, vo, W_O2 + h * 16);
        if (__builtin_amdgcn_ballot_w64(!valid4(yv)) == 0) break;
      }
    }
    {
      float s[4] = {asf(yv[0]), asf(yv[1]), asf(yv[2]), asf(yv[3])};
#pragma unroll
      for (int k = 0; k < 4; ++k) {
        float t = row16_sum(s[k]);
        auto sw = __builtin_amdgcn_permlane16_swap(__float_as_uint(t), __float_as_uint(t), false, false);
        t = __uint_as_float(sw[0]) + __uint_as_float(sw[1]);
        s[k] = xo[k] + rbf(rbf(t) * gt[k]);
      }
      if (lane == 0) *reinterpret_cast<u32x2*>(out + oidx) = u32x2{cvtpk(s[0], s[1]), cvtpk(s[2], s[3])};
    }
    STAMP(3);
    return;
  }

  const int sw = wave - 1;
  const int t0 = fgrp ? (sp == 0 ? 0 : 247 + 259 * (sp - 1)) : 256 * sp;
  const u32 ntile = fgrp ? (sp == 0 ? 247u : 259u) : 256u;
  const size_t kvbase = ((size_t)(b * NH + h) * SK + (size_t)t0 * TILE) * DH;
  const char* kp = reinterpret_cast<const char*>(kc + kvbase);
  const char* vp = reinterpret_cast<const char*>(vc + kvbase);
  const u32 voff = lane * 16;
  const u32 ring = (u32)(uintptr_t)(__attribute__((address_space(3))) float*)lds + sw * RING_W;
  __builtin_amdgcn_s_barrier();
  u32x4 R[16];
  dma4k(kp + (size_t)sw * 4096, voff, ring);
  dma4k(vp + (size_t)sw * 4096, voff, ring + 4096);
  WAITV(4); dma4k(kp + (size_t)(NW + sw) * 4096, voff, ring + 8192);
  WAITV(4); dma4k(vp + (size_t)(NW + sw) * 4096, voff, ring + 12288);
  WAITV(4); ld4k(R, kp + (size_t)(2 * NW + sw) * 4096, voff);
  WAITV(4); ld4k(R + 4, vp + (size_t)(2 * NW + sw) * 4096, voff);
  WAITV(4); ld4k(R + 8, kp + (size_t)(3 * NW + sw) * 4096, voff);
  WAITV(4); ld4k(R + 12, vp + (size_t)(3 * NW + sw) * 4096, voff);
  BAR();
  const u32x4 qpk = *reinterpret_cast<const u32x4*>(lds + L_QK + 4 * c);

  constexpr float SCALE = 0.08838834764831845f;
  float m = -INFINITY, l = 0.f, acc[8];
#pragma unroll
  for (int k = 0; k < 8; ++k) acc[k] = 0.f;
  const bool b0 = (c & 1) != 0, b1 = (c & 2) != 0;
  const char* lbase = reinterpret_cast<const char*>(lds) + sw * RING_W + lane * 16;
  float p0, p1, p2, p3;

  auto score = [&](const u32x4* K) __attribute__((always_inline)) {
    float d[4];
#pragma unroll
    for (int t = 0; t < 4; ++t) {
      float s = dot2(K[t][0], qpk[0], 0.f);
      s = dot2(K[t][1], qpk[1], s); s = dot2(K[t][2], qpk[2], s); s = dot2(K[t][3], qpk[3], s);
      d[t] = s;
    }
    float e0 = b0 ? d[1] : d[0], f0 = b0 ? d[0] : d[1];
    float e1 = b0 ? d[3] : d[2], f1 = b0 ? d[2] : d[3];
    e0 += dpp<XOR1>(f0); e1 += dpp<XOR1>(f1);
    float g0 = b1 ? e1 : e0, g1 = b1 ? e0 : e1;
    g0 += dpp<XOR2>(g1);
    g0 += dpp<ROR4>(g0); g0 += dpp<XOR8>(g0);
    const float sc = rbf(rbf(g0) * SCALE) * LOG2E;
    float tm = fmaxf(sc, dpp<XOR1>(sc)); tm = fmaxf(tm, dpp<XOR2>(tm));
    const float mn = fmaxf(m, tm);
    const float alpha = __builtin_amdgcn_exp2f(m - mn), p = __builtin_amdgcn_exp2f(sc - mn);
    m = mn;
    l = fmaf(l, alpha, p);
    p0 = dpp<0x00>(p); p1 = dpp<0x55>(p); p2 = dpp<0xAA>(p); p3 = dpp<0xFF>(p);
#pragma unroll
    for (int k = 0; k < 8; ++k) acc[k] *= alpha;
  };
  auto accum = [&](const u32x4* V) __attribute__((always_inline)) {
#pragma unroll
    for (int i = 0; i < 4; ++i) {
      const float pt = i == 0 ? p0 : i == 1 ? p1 : i == 2 ? p2 : p3;
#pragma unroll
      for (int k = 0; k < 4; ++k) { acc[2 * k] = fmaf(pt, bflo(V[i][k]), acc[2 * k]); acc[2 * k + 1] = fmaf(pt, bfhi(V[i][k]), acc[2 * k + 1]); }
    }
  };
  auto rd = [&](u32x4* D, u32 off) __attribute__((always_inline)) {
#pragma unroll
    for (int i = 0; i < 4; ++i) D[i] = *reinterpret_cast<const u32x4*>(lbase + off + i * 1024);
  };
  u32* tkp = reinterpret_cast<u32*>(lds) + L_TK;
  auto ticket = [&]() -> u32 { u32 t = 0; if (lane == 0) t = __hip_atomic_fetch_add(tkp, 1u, __ATOMIC_RELAXED, __HIP_MEMORY_SCOPE_WORKGROUP); return t; };

  u32 slot = 0;
  u32 tk = ticket();
#pragma unroll
  for (int s2 = 0; s2 < 2; ++s2) {
    u32x4 D[4];
    WAITV(28);
    rd(D, s2 * 8192);
    WAITL();
    dma4k(kp + (size_t)((4 + s2) * NW + sw) * 4096, voff, ring + s2 * 8192);
    score(D);
    WAITV(28);
    rd(D, s2 * 8192 + 4096);
    WAITL();
    dma4k(vp + (size_t)((4 + s2) * NW + sw) * 4096, voff, ring + s2 * 8192 + 4096);
    accum(D);
  }
  PIN4(28, R); score(R);
  PIN4(24, R + 4); accum(R + 4);
  PIN4(20, R + 8); score(R + 8);
  PIN4(16, R + 12); accum(R + 12);
  for (;;) {
    const u32 t = __builtin_amdgcn_readfirstlane(tk);
    if (t >= ntile) break;
    tk = ticket();
    u32x4 D[4];
    WAITD();
    rd(D, slot);
    WAITL();
    dma4k(kp + (size_t)t * 4096, voff, ring + slot);
    score(D);
    WAITD();
    rd(D, slot + 4096);
    WAITL();
    dma4k(vp + (size_t)t * 4096, voff, ring + slot + 4096);
    accum(D);
    slot ^= 8192;
  }
  {
    u32x4 D[4];
    WAITV(12); rd(D, slot); score(D);
    WAITV(8); rd(D, slot + 4096); accum(D);
    WAITV(4); rd(D, slot ^ 8192); score(D);
    WAITV(0); rd(D, (slot ^ 8192) + 4096); accum(D);
  }
  l += dpp<XOR1>(l); l += dpp<XOR2>(l);
  {
    const float mw = xrow_max(m);
    const float sc = __builtin_amdgcn_exp2f(m - mw);
    l = xrow_sum(l * sc);
#pragma unroll
    for (int k = 0; k < 8; ++k) acc[k] = xrow_sum(acc[k] * sc);
    if (r == 0) {
      *reinterpret_cast<f32x4*>(lds + L_QP + sw * DH + c * 8) = f32x4{acc[0], acc[1], acc[2], acc[3]};
      *reinterpret_cast<f32x4*>(lds + L_QP + sw * DH + c * 8 + 4) = f32x4{acc[4], acc[5], acc[6], acc[7]};
      if (c == 0) { lds[L_MRG + sw * 2] = mw; lds[L_MRG + sw * 2 + 1] = l; }
    }
  }
  BAR();
}

extern "C" void kernel_launch(void* const* d_in, const int* in_sizes, int n_in, void* d_out, int out_size, void* d_ws, size_t ws_size,
                              hipStream_t stream) {
  const __hip_bfloat16* x = (const __hip_bfloat16*)d_in[0];
  const __hip_bfloat16* kc = (const __hip_bfloat16*)d_in[1];
  const __hip_bfloat16* vc = (const __hip_bfloat16*)d_in[2];
  const __hip_bfloat16* wq = (const __hip_bfloat16*)d_in[3];
  const __hip_bfloat16* wo = (const __hip_bfloat16*)d_in[4];
  const __hip_bfloat16* wg = (const __hip_bfloat16*)d_in[6];
  const __hip_bfloat16* wu = (const __hip_bfloat16*)d_in[7];
  const __hip_bfloat16* wd = (const __hip_bfloat16*)d_in[8];
  (void)hipFuncSetAttribute((const void*)fused_k, hipFuncAttributeMaxDynamicSharedMemorySize, L_END * 4);
  fused_k<<<NWG, NT, L_END * 4, stream>>>(x, kc, vc, wq, wo, wg, wu, wd, (char*)d_ws, (__hip_bfloat16*)d_out);
}
```

```cpp
#include <hip/hip_runtime.h>
#include <hip/hip_bf16.h>
#include <stdint.h>

typedef unsigned int u32;
typedef __attribute__((ext_vector_type(4))) unsigned int u32x4;
typedef __attribute__((ext_vector_type(4))) float f32x4;
typedef __attribute__((ext_vector_type(2))) float f32x2;
typedef __attribute__((ext_vector_type(2))) __bf16 bf16x2;

constexpr int NB = 2, DM = 512, NH = 32, SK = 16384, DH = 128, FF = 1376;
constexpr int NSP = 4;
constexpr int NW = 8;
constexpr int NT = (NW + 1) * 64;
constexpr int TOK_WG = SK / NSP;
constexpr int TILE = 16;
constexpr int NTILE = TOK_WG / (TILE * NW);
constexpr int NTWG = NTILE * NW;
constexpr int FSL = 32;
constexpr int NFS = FF / FSL;
constexpr int NWG = NB * NH * NSP;

__device__ __forceinline__ float bflo(u32 v) { return __builtin_bit_cast(float, v << 16); }
__device__ __forceinline__ float bfhi(u32 v) { return __builtin_bit_cast(float, v & 0xffff0000u); }
__device__ __forceinline__ u32 cvtpk(float lo, float hi) {
  u32 r; asm("v_cvt_pk_bf16_f32 %0, %1, %2" : "=v"(r) : "v"(lo), "v"(hi)); return r;
}
__device__ __forceinline__ float rbf(float x) { return bflo(cvtpk(x, x)); }
__device__ __forceinline__ float dot2(u32 k, u32 q, float acc) {
  return __builtin_amdgcn_fdot2_f32_bf16(__builtin_bit_cast(bf16x2, k), __builtin_bit_cast(bf16x2, q), acc, false);
}
template <int CTRL> __device__ __forceinline__ float dpp(float x) {
  return __builtin_bit_cast(float, __builtin_amdgcn_mov_dpp(__builtin_bit_cast(int, x), CTRL, 0xf, 0xf, true));
}
constexpr int XOR1 = 0xB1, XOR2 = 0x4E, XOR7 = 0x141, XOR8 = 0x128, ROR4 = 0x124;
__device__ __forceinline__ float xrow_sum(float x) {
  auto s = __builtin_amdgcn_permlane16_swap(__float_as_uint(x), __float_as_uint(x), false, false);
  x = __uint_as_float(s[0]) + __uint_as_float(s[1]);
  auto t = __builtin_amdgcn_permlane32_swap(__float_as_uint(x), __float_as_uint(x), false, false);
  return __uint_as_float(t[0]) + __uint_as_float(t[1]);
}
__device__ __forceinline__ float xrow_max(float x) {
  auto s = __builtin_amdgcn_permlane16_swap(__float_as_uint(x), __float_as_uint(x), false, false);
  x = fmaxf(__uint_as_float(s[0]), __uint_as_float(s[1]));
  auto t = __builtin_amdgcn_permlane32_swap(__float_as_uint(x), __float_as_uint(x), false, false);
  return fmaxf(__uint_as_float(t[0]), __uint_as_float(t[1]));
}
__device__ __forceinline__ float row16_sum(float x) {
  x += dpp<XOR1>(x); x += dpp<XOR2>(x); x += dpp<ROR4>(x); x += dpp<XOR8>(x); return x;
}
__device__ __forceinline__ float wave_sum(float x) { return xrow_sum(row16_sum(x)); }
__device__ __forceinline__ u32x4 ldnt(const void* p) { return __builtin_nontemporal_load(reinterpret_cast<const u32x4*>(p)); }
__device__ __forceinline__ u32x4 ld16(const void* p) { return *reinterpret_cast<const u32x4*>(p); }

typedef unsigned long long u64;
constexpr u32 W_QD = 0;
constexpr u32 W_O1 = 2u << 20;
constexpr u32 W_O2 = W_O1 + (256 << 10);
constexpr u32 W_Z = W_O1 + (512 << 10);
constexpr u32 W_CH = 3u << 20;
constexpr u32 W_R = W_CH + (32 << 10);
constexpr u32 W_LCG = W_R + (4 << 10);
constexpr u32 W_END = W_LCG + (4 << 10);
constexpr u32 SENT = 0xffffffffu;
constexpr int LEADER = 255;

typedef __attribute__((ext_vector_type(2))) unsigned int u32x2;
typedef __amdgpu_buffer_rsrc_t rsrc_t;
#define MKRS(p, bytes) __builtin_amdgcn_make_buffer_rsrc((void*)(p), 0, (int)(bytes), 0x00020000)
__device__ __forceinline__ u32x4 cl4(rsrc_t rs, u32 v, u32 so) { return __builtin_amdgcn_raw_buffer_load_b128(rs, (int)v, (int)so, 18); }
__device__ __forceinline__ u32x2 cl2(rsrc_t rs, u32 v, u32 so) { return __builtin_amdgcn_raw_buffer_load_b64(rs, (int)v, (int)so, 18); }
__device__ __forceinline__ u32 cl1(rsrc_t rs, u32 v, u32 so) { return __builtin_amdgcn_raw_buffer_load_b32(rs, (int)v, (int)so, 18); }
__device__ __forceinline__ void cs4(rsrc_t rs, u32 v, u32 so, u32x4 d) { __builtin_amdgcn_raw_buffer_store_b128(d, rs, (int)v, (int)so, 16); }
__device__ __forceinline__ void cs2(rsrc_t rs, u32 v, u32 so, u32x2 d) { __builtin_amdgcn_raw_buffer_store_b64(d, rs, (int)v, (int)so, 16); }
__device__ __forceinline__ void cs1(rsrc_t rs, u32 v, u32 so, u32 d) { __builtin_amdgcn_raw_buffer_store_b32(d, rs, (int)v, (int)so, 16); }
__device__ __forceinline__ u32x4 wl4(rsrc_t rs, u32 v, u32 so) { return __builtin_amdgcn_raw_buffer_load_b128(rs, (int)v, (int)so, 2); }
__device__ __forceinline__ u32 wl1(rsrc_t rs, u32 v, u32 so) { return __builtin_amdgcn_raw_buffer_load_b32(rs, (int)v, (int)so, 2); }
#define FENCE() asm volatile("" ::: "memory")
#define POLL() asm volatile("" ::: "memory")
__device__ __forceinline__ u32 canon(float f) { const u32 v = __float_as_uint(f); return v == SENT ? 0x7fffffffu : v; }
__device__ __forceinline__ float asf(u32 v) { return __uint_as_float(v); }
__device__ __forceinline__ bool valid4(u32x4 v) { return (v[0] != SENT) & (v[1] != SENT) & (v[2] != SENT) & (v[3] != SENT); }
__device__ __forceinline__ int wgid(int h, int j) { return (h >> 2) + 8 * (((h & 3) << 3) + j); }

#define STAMP(i) do {} while (0)
#define THR 1
constexpr int RING_W = 16384;
constexpr int L_XN = NW * RING_W / 4;
constexpr int L_QP = L_XN + 2 * DM;
constexpr int L_MRG = L_QP + NW * DH;
constexpr int L_QK = L_MRG + 16;
constexpr int L_OP = L_QK + 64;
constexpr int L_FP = L_OP + 128;
constexpr int L_HH = L_FP + 128;
constexpr int L_TK = L_HH + 64;
constexpr int L_END = L_TK + 4;

__device__ __forceinline__ float gelu_tanh(float x) {
  const float k0 = 0.7978845608028654f, k1 = 0.044715f;
  float u = k0 * (x + k1 * x * x * x);
  return 0.5f * x * (1.0f + tanhf(u));
}
__device__ __forceinline__ void dma4k(const void* sbase, u32 voff, u32 ldst) {
  u32 keep;
  asm volatile("s_mov_b32 %0, m0\n\ts_mov_b32 m0, %3\n\ts_nop 0\n\t"
               "global_load_lds_dwordx4 %1, %2 nt\n\t"
               "global_load_lds_dwordx4 %1, %2 offset:1024 nt\n\t"
               "global_load_lds_dwordx4 %1, %2 offset:2048 nt\n\t"
               "global_load_lds_dwordx4 %1, %2 offset:3072 nt\n\t"
               "s_mov_b32 m0, %0"
               : "=&s"(keep) : "v"(voff), "s"(sbase), "s"(ldst) : "memory");
}
__device__ __forceinline__ void ld4k(u32x4* R, const void* sbase, u32 voff) {
  asm volatile("global_load_dwordx4 %0, %4, %5 nt\n\t"
               "global_load_dwordx4 %1, %4, %5 offset:1024 nt\n\t"
               "global_load_dwordx4 %2, %4, %5 offset:2048 nt\n\t"
               "global_load_dwordx4 %3, %4, %5 offset:3072 nt"
               : "=&v"(R[0]), "=&v"(R[1]), "=&v"(R[2]), "=&v"(R[3]) : "v"(voff), "s"(sbase) : "memory");
}
#define PIN4(n, a) asm volatile("s_waitcnt vmcnt(" #n ")" : "+v"((a)[0]), "+v"((a)[1]), "+v"((a)[2]), "+v"((a)[3]) :: "memory")
#define WAITV(n) asm volatile("s_waitcnt vmcnt(" #n ")" ::: "memory")
#define BK1 99
#define SL1 48
#define BK2 99
#define SL2 48
#define FD 7
#define DEPTH 4
#define WAITV2(n) WAITV(n)
#define WAITD() WAITV2(DEPTH)
#define WAITL() asm volatile("s_waitcnt lgkmcnt(0)" ::: "memory")
#define BAR() do { WAITL(); __builtin_amdgcn_s_barrier(); asm volatile("" ::: "memory"); } while (0)

__global__ void __launch_bounds__(NT) fused_k(const __hip_bfloat16* __restrict__ x, const __hip_bfloat16* __restrict__ kc,
                                               const __hip_bfloat16* __restrict__ vc, const __hip_bfloat16* __restrict__ wq,
                                               const __hip_bfloat16* __restrict__ wo, const __hip_bfloat16* __restrict__ wg,
                                               const __hip_bfloat16* __restrict__ wu, const __hip_bfloat16* __restrict__ wd,
                                               char* ws, __hip_bfloat16* __restrict__ out) {
  extern __shared__ __attribute__((aligned(16))) float lds[];
  const int tid = threadIdx.x, lane = tid & 63, wave = __builtin_amdgcn_readfirstlane(tid >> 6);
  const int r = lane >> 4, c = lane & 15;
  const int bid = blockIdx.x;
  const int xcd = bid & 7, j = (bid >> 3) & 7, hl = bid >> 6;
  const int h = xcd * 4 + hl, b = j >> 2, sp = j & 3;
  const int fe = (hl * 2 + b) * 8 + xcd;
  const bool fgrp = fe < NFS;
  constexpr float LOG2E = 1.4426950408889634f;

  if (wave == 0) {
    __builtin_amdgcn_s_setprio(3);
    STAMP(0);
    const rsrc_t rw = MKRS(ws, W_END);
    const u32 l16 = lane * 16, l8 = lane * 8;
    int spin = 0; (void)spin;
    const u32 inbox = W_QD + bid * 8192;
    const u32 T0 = (u32)(__builtin_amdgcn_s_memrealtime() >> 11);
    const u32 chdst = wgid(h, lane & 7) * 128;
    if (lane < 8 && lane != j) cs1(rw, chdst, W_CH + j * 4, T0);
    const u32x4 xr0 = ld16(x + lane * 8), xr1 = ld16(x + DM + lane * 8);
    u32x4 wqr[16];
    {
      const rsrc_t rq = MKRS(wq, DM * NH * DH * 2);
      const u32 vo = (r * 16 * (NH * DH) + c * 8) * 2;
#pragma unroll
      for (int i = 0; i < 16; ++i) wqr[i] = wl4(rq, vo, ((j * 64 + i) * (NH * DH) + h * DH) * 2);
    }
    u32x4 g[8];
#pragma unroll
    for (int jp = 0; jp < 8; ++jp) g[jp] = cl4(rw, l16, inbox + jp * 1024);
    const u32 g0v = cl1(rw, 0, W_LCG + bid * 8 + 4);
    u32 le[4] = {0, 0, 0, 0};
    if (bid == LEADER) {
#pragma unroll
      for (int k = 0; k < 4; ++k) le[k] = cl1(rw, l8, W_R + 512 * k);
    }
    if (lane == 0) reinterpret_cast<u32*>(lds)[L_TK + 1] = 0;
    BAR();
    STAMP(4);
#pragma unroll
    for (int bb = 0; bb < 2; ++bb) {
      const u32x4 xv = bb ? xr1 : xr0;
      float e[8];
#pragma unroll
      for (int k = 0; k < 4; ++k) { e[2 * k] = bflo(xv[k]); e[2 * k + 1] = bfhi(xv[k]); }
      float s = 0.f;
#pragma unroll
      for (int k = 0; k < 8; ++k) s += e[k];
      const float mean = wave_sum(s) * (1.0f / DM);
      float q2 = 0.f;
#pragma unroll
      for (int k = 0; k < 8; ++k) { const float d = e[k] - mean; q2 = fmaf(d, d, q2); }
      const float var = rbf(wave_sum(q2) * (1.0f / DM));
      const float mb = rbf(mean);
      const float den = rbf(sqrtf(rbf(var + 1e-5f)));
      float o[8];
#pragma unroll
      for (int k = 0; k < 8; ++k) o[k] = rbf(rbf(e[k] - mb) / den);
      *reinterpret_cast<f32x4*>(lds + L_XN + bb * DM + lane * 8) = f32x4{o[0], o[1], o[2], o[3]};
      *reinterpret_cast<f32x4*>(lds + L_XN + bb * DM + lane * 8 + 4) = f32x4{o[4], o[5], o[6], o[7]};
    }
    float v[2][2];
    {
      float a[2][8];
#pragma unroll
      for (int bb = 0; bb < 2; ++bb)
#pragma unroll
        for (int k = 0; k < 8; ++k) a[bb][k] = 0.f;
#pragma unroll
      for (int i4 = 0; i4 < 4; ++i4) {
        const f32x4 xa = *reinterpret_cast<const f32x4*>(lds + L_XN + j * 64 + r * 16 + i4 * 4);
        const f32x4 xb = *reinterpret_cast<const f32x4*>(lds + L_XN + DM + j * 64 + r * 16 + i4 * 4);
#pragma unroll
        for (int i = 0; i < 4; ++i)
#pragma unroll
          for (int k = 0; k < 4; ++k) {
            const float wl = bflo(wqr[i4 * 4 + i][k]), wh = bfhi(wqr[i4 * 4 + i][k]);
            a[0][2 * k] = fmaf(xa[i], wl, a[0][2 * k]); a[0][2 * k + 1] = fmaf(xa[i], wh, a[0][2 * k + 1]);
            a[1][2 * k] = fmaf(xb[i], wl, a[1][2 * k]); a[1][2 * k + 1] = fmaf(xb[i], wh, a[1][2 * k + 1]);
          }
      }
#pragma unroll
      for (int bb = 0; bb < 2; ++bb) {
        float t[4];
#pragma unroll
        for (int k = 0; k < 4; ++k) {
          auto sw = __builtin_amdgcn_permlane32_swap(__float_as_uint(a[bb][k]), __float_as_uint(a[bb][4 + k]), false, false);
          t[k] = asf(sw[0]) + asf(sw[1]);
        }
#pragma unroll
        for (int e = 0; e < 2; ++e) {
          auto sw = __builtin_amdgcn_permlane16_swap(__float_as_uint(t[e]), __float_as_uint(t[2 + e]), false, false);
          v[bb][e] = asf(sw[0]) + asf(sw[1]);
        }
      }
    }
    STAMP(5);
#pragma unroll
    for (int jc = 0; jc < 8; ++jc)
      if (jc != j) cs4(rw, l16, W_QD + (wgid(h, jc) * 8 + j) * 1024, u32x4{__float_as_uint(v[jc >> 2][0]), T0, __float_as_uint(v[jc >> 2][1]), T0});
    u32 Tlo = T0 - 1u, Tspan = 2u;
    for (;;) {
      bool hit = false;
#pragma unroll
      for (int jp = 0; jp < 8; ++jp) hit |= (g[jp][1] - Tlo <= Tspan) | (g[jp][3] - Tlo <= Tspan);
      if (__builtin_amdgcn_ballot_w64(hit) == 0) break;
      Tlo += Tspan + 1u; Tspan = 0u;
    }
    if (Tspan == 0u && lane < 8 && lane != j) cs1(rw, chdst, W_CH + j * 4, Tlo);
    const u32 gch = __builtin_amdgcn_readfirstlane(g0v) + 1u;
    u32 last[8];
#pragma unroll
    for (int k = 0; k < 8; ++k) last[k] = T0;
    const u32 chv_off = (lane & 7) * 4, chs_off = W_CH + bid * 128;
    auto service = [&](u32 chv) __attribute__((always_inline)) {
#pragma unroll
      for (int jc = 0; jc < 8; ++jc) {
        const u32 t = __builtin_amdgcn_readlane(chv, jc);
        if (jc != j && t != last[jc]) {
          last[jc] = t;
          cs4(rw, l16, W_QD + (wgid(h, jc) * 8 + j) * 1024, u32x4{__float_as_uint(v[jc >> 2][0]), t, __float_as_uint(v[jc >> 2][1]), t});
        }
      }
    };
    u32 miss = 0xffu & ~(1u << j);
    for (int it = 0;;) {
      POLL();
      const u32 chv = cl1(rw, chv_off, chs_off);
#pragma unroll
      for (int jp = 0; jp < 8; ++jp) if (miss >> jp & 1) g[jp] = cl4(rw, l16, inbox + jp * 1024);
#pragma unroll
      for (int jp = 0; jp < 8; ++jp)
        if ((miss >> jp & 1) && __builtin_amdgcn_ballot_w64((g[jp][1] - Tlo > Tspan) | (g[jp][3] - Tlo > Tspan)) == 0) miss &= ~(1u << jp);
      if (miss == 0) break;
      if (it >= 3) service(chv); else ++it;
    }
    {
      const float own0 = b ? v[1][0] : v[0][0], own1 = b ? v[1][1] : v[0][1];
      float q0 = 0.f, q1 = 0.f;
#pragma unroll
      for (int jp = 0; jp < 8; ++jp) { q0 += (jp == j) ? own0 : asf(g[jp][0]); q1 += (jp == j) ? own1 : asf(g[jp][2]); }
      reinterpret_cast<u32*>(lds)[L_QK + 4 * c + r] = cvtpk(q0, q1);
      if (lane == 0) reinterpret_cast<u32*>(lds)[L_TK] = 6 * NW;
    }
    WAITL();
    if (lane == 0) __hip_atomic_store(reinterpret_cast<u32*>(lds) + L_TK + 1, 1u, __ATOMIC_RELAXED, __HIP_MEMORY_SCOPE_WORKGROUP);
    FENCE();
    STAMP(1);
    __builtin_amdgcn_s_setprio(0);
    cs4(rw, l16, W_O1 + bid * 1024, u32x4{SENT, SENT, SENT, SENT});
    cs2(rw, l8, W_O2 + bid * 512, u32x2{SENT, SENT});
    if (fgrp && sp == 0) {
#pragma unroll
      for (int k = 0; k < 4; ++k) cs4(rw, l16, W_Z + fe * (2 * DM * 4) + k * 1024, u32x4{SENT, SENT, SENT, SENT});
    }
    if (bid == LEADER) {
#pragma unroll
      for (int k = 0; k < 4; ++k) { le[k] += 1u; cs1(rw, l8, W_LCG + 512 * k, le[k]); }
    }
    WAITV(0);
    {
      u32 lastlc = 0; bool f1 = true, ldone = false;
      for (;;) {
        POLL();
        const u32x2 lcg = cl2(rw, 0, W_LCG + bid * 8);
        const u32 chv = cl1(rw, chv_off, chs_off);
        u32x2 rr4[4];
        if (bid == LEADER) {
#pragma unroll
          for (int k = 0; k < 4; ++k) rr4[k] = cl2(rw, l8, W_R + 512 * k);
        }
        const u32 lc = __builtin_amdgcn_readfirstlane(lcg[0]), lg = __builtin_amdgcn_readfirstlane(lcg[1]);
        if (lg == gch) break;
        if (f1 || lc != lastlc) { if (lane == 0) cs2(rw, 0, W_R + bid * 8, u32x2{lc, gch}); lastlc = lc; f1 = false; }
        service(chv);
        if (bid == LEADER && !ldone) {
          bool bad = false;
#pragma unroll
          for (int k = 0; k < 4; ++k) bad |= rr4[k][0] != le[k];
          if (__builtin_amdgcn_ballot_w64(bad) == 0) {
#pragma unroll
            for (int k = 0; k < 4; ++k) cs1(rw, l8, W_LCG + 512 * k + 4, rr4[k][1]);
            ldone = true;
          }
        }
        __builtin_amdgcn_s_sleep(16);
      }
    }
    STAMP(6);
    if (fgrp && sp == 0) {
      const int f0 = fe * FSL, rr = lane >> 2, cc = lane & 3;
      float ag[2][8], au[2][8];
#pragma unroll
      for (int bb = 0; bb < 2; ++bb)
#pragma unroll
        for (int k = 0; k < 8; ++k) { ag[bb][k] = 0.f; au[bb][k] = 0.f; }
      const rsrc_t rg = MKRS(wg, DM * FF * 2), ru = MKRS(wu, DM * FF * 2), rd = MKRS(wd, DM * FF * 2);
      const u32 vo = (rr * 32 * FF + cc * 8) * 2;
#pragma unroll 1
      for (int ch = 0; ch < 4; ++ch) {
        u32x4 fg[8], fu[8];
#pragma unroll
        for (int i = 0; i < 8; ++i) { const u32 so = ((ch * 8 + i) * FF + f0) * 2; fg[i] = wl4(rg, vo, so); fu[i] = wl4(ru, vo, so); }
#pragma unroll
        for (int i4 = 0; i4 < 2; ++i4) {
          const f32x4 xa = *reinterpret_cast<const f32x4*>(lds + L_XN + rr * 32 + ch * 8 + i4 * 4);
          const f32x4 xb = *reinterpret_cast<const f32x4*>(lds + L_XN + DM + rr * 32 + ch * 8 + i4 * 4);
#pragma unroll
          for (int i = 0; i < 4; ++i)
#pragma unroll
            for (int k = 0; k < 4; ++k) {
              const float gl = bflo(fg[i4 * 4 + i][k]), gh = bfhi(fg[i4 * 4 + i][k]), ul = bflo(fu[i4 * 4 + i][k]), uh = bfhi(fu[i4 * 4 + i][k]);
              ag[0][2 * k] = fmaf(xa[i], gl, ag[0][2 * k]); ag[0][2 * k + 1] = fmaf(xa[i], gh, ag[0][2 * k + 1]);
              ag[1][2 * k] = fmaf(xb[i], gl, ag[1][2 * k]); ag[1][2 * k + 1] = fmaf(xb[i], gh, ag[1][2 * k + 1]);
              au[0][2 * k] = fmaf(xa[i], ul, au[0][2 * k]); au[0][2 * k + 1] = fmaf(xa[i], uh, au[0][2 * k + 1]);
              au[1][2 * k] = fmaf(xb[i], ul, au[1][2 * k]); au[1][2 * k + 1] = fmaf(xb[i], uh, au[1][2 * k + 1]);
            }
        }
      }
#pragma unroll
      for (int bb = 0; bb < 2; ++bb) {
#pragma unroll
        for (int k = 0; k < 8; ++k) {
          float t2 = ag[bb][k]; t2 += dpp<ROR4>(t2); t2 += dpp<XOR8>(t2); ag[bb][k] = xrow_sum(t2);
          float u = au[bb][k]; u += dpp<ROR4>(u); u += dpp<XOR8>(u); au[bb][k] = xrow_sum(u);
        }
        if (lane < 4) {
          float* pg = lds + L_FP + bb * 32 + lane * 8;
          float* pu = lds + L_FP + 64 + bb * 32 + lane * 8;
          *reinterpret_cast<f32x4*>(pg) = f32x4{ag[bb][0], ag[bb][1], ag[bb][2], ag[bb][3]};
          *reinterpret_cast<f32x4*>(pg + 4) = f32x4{ag[bb][4], ag[bb][5], ag[bb][6], ag[bb][7]};
          *reinterpret_cast<f32x4*>(pu) = f32x4{au[bb][0], au[bb][1], au[bb][2], au[bb][3]};
          *reinterpret_cast<f32x4*>(pu + 4) = f32x4{au[bb][4], au[bb][5], au[bb][6], au[bb][7]};
        }
      }
      WAITL();
      {
        const float gl = rbf(gelu_tanh(rbf(lds[L_FP + lane])));
        lds[L_HH + lane] = rbf(gl * rbf(lds[L_FP + 64 + lane]));
      }
      WAITL();
      float z[2][8];
#pragma unroll
      for (int bb = 0; bb < 2; ++bb)
#pragma unroll
        for (int k = 0; k < 8; ++k) z[bb][k] = 0.f;
#pragma unroll 1
      for (int ch = 0; ch < 2; ++ch) {
        u32x4 fd[16];
#pragma unroll
        for (int i = 0; i < 16; ++i) fd[i] = wl4(rd, l16, (f0 + ch * 16 + i) * (DM * 2));
#pragma unroll
        for (int i4 = 0; i4 < 4; ++i4) {
          const f32x4 ha = *reinterpret_cast<const f32x4*>(lds + L_HH + ch * 16 + i4 * 4);
          const f32x4 hb = *reinterpret_cast<const f32x4*>(lds + L_HH + 32 + ch * 16 + i4 * 4);
#pragma unroll
          for (int i = 0; i < 4; ++i)
#pragma unroll
            for (int k = 0; k < 4; ++k) {
              const float wl = bflo(fd[i4 * 4 + i][k]), wh = bfhi(fd[i4 * 4 + i][k]);
              z[0][2 * k] = fmaf(ha[i], wl, z[0][2 * k]); z[0][2 * k + 1] = fmaf(ha[i], wh, z[0][2 * k + 1]);
              z[1][2 * k] = fmaf(hb[i], wl, z[1][2 * k]); z[1][2 * k + 1] = fmaf(hb[i], wh, z[1][2 * k + 1]);
            }
        }
      }
#pragma unroll
      for (int bb = 0; bb < 2; ++bb)
#pragma unroll
        for (int k = 0; k < 2; ++k)
          cs4(rw, lane * 32, W_Z + (fe * 2 + bb) * (DM * 4) + k * 16,
              u32x4{canon(z[bb][4 * k]), canon(z[bb][4 * k + 1]), canon(z[bb][4 * k + 2]), canon(z[bb][4 * k + 3])});
    }
    STAMP(10);
    u32 P0[32], P1[32];
    {
      const rsrc_t ro = MKRS(wo, NH * DH * DM * 2);
      const u32 vo = ((lane >> 5) * 64 * DM + (lane & 31) * 2) * 2, so = (h * DH * DM + j * 64) * 2;
#pragma unroll
      for (int k = 0; k < 32; ++k) { P0[k] = wl1(ro, vo, so + (2 * k) * (DM * 2)); P1[k] = wl1(ro, vo, so + (2 * k + 1) * (DM * 2)); }
#pragma unroll
      for (int k = 0; k < 32; ++k) {
        const u32 A = P0[k], B = P1[k];
        P0[k] = __builtin_amdgcn_perm(B, A, 0x05040100u); P1[k] = __builtin_amdgcn_perm(B, A, 0x07060302u);
        asm volatile("" : "+v"(P0[k]), "+v"(P1[k]));
      }
    }
    const int oidx = (h >> 4) * DM + j * 64 + (h & 15) * 4;
    float gt[4], xo[4];
    {
      u32x4 zv = {0, 0, 0, 0};
      u64 want = (1ull << NFS) - 1;
      for (;;) {
        POLL();
        if (want >> lane & 1) zv = cl4(rw, lane * (2 * DM * 4), W_Z + oidx * 4);
        const u64 bad = __builtin_amdgcn_ballot_w64(!valid4(zv));
        if (bad == 0) break;
        if (bad & want) { want = bad & (0 - bad); __builtin_amdgcn_s_sleep(64); } else want = bad;
      }
      const u32x2 xv = *reinterpret_cast<const u32x2*>(x + oidx);
#pragma unroll
      for (int k = 0; k < 4; ++k) { const float f = rbf(wave_sum(asf(zv[k]))); gt[k] = rbf(f / (1.0f + __expf(-f))); }
      xo[0] = bflo(xv[0]); xo[1] = bfhi(xv[0]); xo[2] = bflo(xv[1]); xo[3] = bfhi(xv[1]);
    }
    STAMP(8);
    __builtin_amdgcn_s_setprio(3);
    BAR();
    float oM, oL, o0 = 0.f, o1 = 0.f;
    {
      f32x4 ml[4];
#pragma unroll
      for (int i = 0; i < 4; ++i) ml[i] = *reinterpret_cast<const f32x4*>(lds + L_MRG + i * 4);
      oM = fmaxf(fmaxf(fmaxf(ml[0][0], ml[0][2]), fmaxf(ml[1][0], ml[1][2])), fmaxf(fmaxf(ml[2][0], ml[2][2]), fmaxf(ml[3][0], ml[3][2])));
      oL = 0.f;
#pragma unroll
      for (int w = 0; w < NW; ++w) {
        const float wt = __builtin_amdgcn_exp2f(ml[w >> 1][(w & 1) * 2] - oM);
        oL = fmaf(wt, ml[w >> 1][(w & 1) * 2 + 1], oL);
        const f32x2 pv = *reinterpret_cast<const f32x2*>(lds + L_QP + w * DH + lane * 2);
        o0 = fmaf(wt, pv[0], o0); o1 = fmaf(wt, pv[1], o1);
      }
    }
    cs4(rw, l16, W_O1 + bid * 1024, u32x4{canon(o0), canon(o1), canon(oM), canon(oL)});
    STAMP(2);
    miss = 0xffu & ~(1u << j);
    for (;;) {
      POLL();
#pragma unroll
      for (int jp = 0; jp < 8; ++jp) if (miss >> jp & 1) g[jp] = cl4(rw, l16, W_O1 + wgid(h, jp) * 1024);
#pragma unroll
      for (int jp = 0; jp < 8; ++jp)
        if ((miss >> jp & 1) && __builtin_amdgcn_ballot_w64(!valid4(g[jp])) == 0) miss &= ~(1u << jp);
      if (miss == 0) break;
      if (__builtin_popcount(miss) >= BK1) __builtin_amdgcn_s_sleep(SL1);
    }
    STAMP(7);
#pragma unroll
    for (int bb = 0; bb < 2; ++bb) {
      float pm[4], pl[4], p0[4], p1[4];
#pragma unroll
      for (int s = 0; s < 4; ++s) {
        const int jp = bb * 4 + s; const bool own = jp == j;
        p0[s] = own ? o0 : asf(g[jp][0]); p1[s] = own ? o1 : asf(g[jp][1]); pm[s] = own ? oM : asf(g[jp][2]); pl[s] = own ? oL : asf(g[jp][3]);
      }
      const float Mb = fmaxf(fmaxf(pm[0], pm[1]), fmaxf(pm[2], pm[3]));
      float Lb = 0.f, a0 = 0.f, a1 = 0.f;
#pragma unroll
      for (int s = 0; s < 4; ++s) { const float wt = __builtin_amdgcn_exp2f(pm[s] - Mb); Lb = fmaf(wt, pl[s], Lb); a0 = fmaf(wt, p0[s], a0); a1 = fmaf(wt, p1[s], a1); }
      const float inv = 1.0f / Lb;
      reinterpret_cast<u32*>(lds)[L_OP + bb * 64 + lane] = cvtpk(a0 * inv, a1 * inv);
    }
    WAITL();
    float y[2][2] = {{0.f, 0.f}, {0.f, 0.f}};
#pragma unroll
    for (int k4 = 0; k4 < 8; ++k4)
#pragma unroll
      for (int bb = 0; bb < 2; ++bb) {
        const u32x4 ov = *reinterpret_cast<const u32x4*>(lds + L_OP + bb * 64 + (lane >> 5) * 32 + k4 * 4);
#pragma unroll
        for (int k = 0; k < 4; ++k) { y[bb][0] = dot2(P0[k4 * 4 + k], ov[k], y[bb][0]); y[bb][1] = dot2(P1[k4 * 4 + k], ov[k], y[bb][1]); }
      }
    {
      auto s0 = __builtin_amdgcn_permlane32_swap(__float_as_uint(y[0][0]), __float_as_uint(y[1][0]), false, false);
      auto s1 = __builtin_amdgcn_permlane32_swap(__float_as_uint(y[0][1]), __float_as_uint(y[1][1]), false, false);
      cs2(rw, l8, W_O2 + bid * 512, u32x2{canon(__uint_as_float(s0[0]) + __uint_as_float(s0[1])), canon(__uint_as_float(s1[0]) + __uint_as_float(s1[1]))});
    }
    u32x4 yv = {0, 0, 0, 0};
    {
      const u32 vo = wgid(lane & 31, j) * 512;
      bool need = lane < 32;
      for (;;) {
        POLL();
        if (need) { yv = cl4(rw, vo, W_O2 + h * 16); need = !valid4(yv); }
        const u64 bad = __builtin_amdgcn_ballot_w64(need);
        if (bad == 0) break;
        if (__builtin_popcountll(bad) >= BK2) __builtin_amdgcn_s_sleep(SL2);
      }
    }
    {
      float s[4] = {asf(yv[0]), asf(yv[1]), asf(yv[2]), asf(yv[3])};
#pragma unroll
      for (int k = 0; k < 4; ++k) {
        float t = row16_sum(s[k]);
        auto sw = __builtin_amdgcn_permlane16_swap(__float_as_uint(t), __float_as_uint(t), false, false);
        t = __uint_as_float(sw[0]) + __uint_as_float(sw[1]);
        s[k] = xo[k] + rbf(rbf(t) * gt[k]);
      }
      if (lane == 0) *reinterpret_cast<u32x2*>(out + oidx) = u32x2{cvtpk(s[0], s[1]), cvtpk(s[2], s[3])};
    }
    STAMP(3);
    return;
  }

  const int sw = wave - 1;
  const int t0 = fgrp ? (sp == 0 ? 0 : (256 - 3 * FD) + (256 + FD) * (sp - 1)) : 256 * sp;
  const u32 ntile = fgrp ? (sp == 0 ? 256u - 3 * FD : 256u + FD) : 256u;
  const size_t kvbase = ((size_t)(b * NH + h) * SK + (size_t)t0 * TILE) * DH;
  const char* kp = reinterpret_cast<const char*>(kc + kvbase);
  const char* vp = reinterpret_cast<const char*>(vc + kvbase);
  const u32 rot = (u32)((bid >> 3) * 8) % ntile;
  auto toff = [&](u32 t) -> size_t { u32 u = t + rot; if (u >= ntile) u -= ntile; return (size_t)u * 4096; };
  const u32 voff = lane * 16;
  const u32 ring = (u32)(uintptr_t)(__attribute__((address_space(3))) float*)lds + sw * RING_W;
  __builtin_amdgcn_s_barrier();
  u32x4 R[16];
  dma4k(kp + toff(sw), voff, ring);
  dma4k(vp + toff(sw), voff, ring + 4096);
  WAITV(4); dma4k(kp + toff((NW + sw)), voff, ring + 8192);
  WAITV(4); dma4k(vp + toff((NW + sw)), voff, ring + 12288);
  WAITV(4); ld4k(R, kp + toff((2 * NW + sw)), voff);
  WAITV(4); ld4k(R + 4, vp + toff((2 * NW + sw)), voff);
  WAITV(4); ld4k(R + 8, kp + toff((3 * NW + sw)), voff);
  WAITV(4); ld4k(R + 12, vp + toff((3 * NW + sw)), voff);
  for (;;) {
    FENCE();
    if (__builtin_amdgcn_readfirstlane(__hip_atomic_load(reinterpret_cast<u32*>(lds) + L_TK + 1, __ATOMIC_RELAXED, __HIP_MEMORY_SCOPE_WORKGROUP)) != 0) break;
    __builtin_amdgcn_s_sleep(2);
  }
  FENCE();
  const u32x4 qpk = *reinterpret_cast<const u32x4*>(lds + L_QK + 4 * c);

  constexpr float SCALE = 0.08838834764831845f;
  float m = -INFINITY, l = 0.f, acc[8];
#pragma unroll
  for (int k = 0; k < 8; ++k) acc[k] = 0.f;
  const bool b0 = (c & 1) != 0, b1 = (c & 2) != 0;
  const char* lbase = reinterpret_cast<const char*>(lds) + sw * RING_W + lane * 16;
  float p0, p1, p2, p3;

  auto score = [&](const u32x4* K) __attribute__((always_inline)) {
    float d[4];
#pragma unroll
    for (int t = 0; t < 4; ++t) {
      float s = dot2(K[t][0], qpk[0], 0.f);
      s = dot2(K[t][1], qpk[1], s); s = dot2(K[t][2], qpk[2], s); s = dot2(K[t][3], qpk[3], s);
      d[t] = s;
    }
    float e0 = b0 ? d[1] : d[0], f0 = b0 ? d[0] : d[1];
    float e1 = b0 ? d[3] : d[2], f1 = b0 ? d[2] : d[3];
    e0 += dpp<XOR1>(f0); e1 += dpp<XOR1>(f1);
    float g0 = b1 ? e1 : e0, g1 = b1 ? e0 : e1;
    g0 += dpp<XOR2>(g1);
    g0 += dpp<ROR4>(g0); g0 += dpp<XOR8>(g0);
    const float sc = rbf(rbf(g0) * SCALE) * LOG2E;
    float tm = fmaxf(sc, dpp<XOR1>(sc)); tm = fmaxf(tm, dpp<XOR2>(tm));
    const float mn = fmaxf(m, tm);
    const float alpha = __builtin_amdgcn_exp2f(m - mn), p = __builtin_amdgcn_exp2f(sc - mn);
    m = mn;
    l = fmaf(l, alpha, p);
    p0 = dpp<0x00>(p); p1 = dpp<0x55>(p); p2 = dpp<0xAA>(p); p3 = dpp<0xFF>(p);
#pragma unroll
    for (int k = 0; k < 8; ++k) acc[k] *= alpha;
  };
  auto accum = [&](const u32x4* V) __attribute__((always_inline)) {
#pragma unroll
    for (int i = 0; i < 4; ++i) {
      const float pt = i == 0 ? p0 : i == 1 ? p1 : i == 2 ? p2 : p3;
#pragma unroll
      for (int k = 0; k < 4; ++k) { acc[2 * k] = fmaf(pt, bflo(V[i][k]), acc[2 * k]); acc[2 * k + 1] = fmaf(pt, bfhi(V[i][k]), acc[2 * k + 1]); }
    }
  };
  auto rd = [&](u32x4* D, u32 off) __attribute__((always_inline)) {
#pragma unroll
    for (int i = 0; i < 4; ++i) D[i] = *reinterpret_cast<const u32x4*>(lbase + off + i * 1024);
  };
  u32* tkp = reinterpret_cast<u32*>(lds) + L_TK;
  auto ticket = [&]() -> u32 { u32 t = 0; if (lane == 0) t = __hip_atomic_fetch_add(tkp, 1u, __ATOMIC_RELAXED, __HIP_MEMORY_SCOPE_WORKGROUP); return t; };

  u32 slot = 0;
  u32 tk = ticket();
#pragma unroll
  for (int s2 = 0; s2 < 2; ++s2) {
    u32x4 D[4];
    WAITV(28);
    rd(D, s2 * 8192);
    WAITL();
    dma4k(kp + toff(((4 + s2) * NW + sw)), voff, ring + s2 * 8192);
    score(D);
    WAITV(28);
    rd(D, s2 * 8192 + 4096);
    WAITL();
    dma4k(vp + toff(((4 + s2) * NW + sw)), voff, ring + s2 * 8192 + 4096);
    accum(D);
  }
  PIN4(28, R); score(R);
  PIN4(24, R + 4); accum(R + 4);
  PIN4(20, R + 8); score(R + 8);
  PIN4(16, R + 12); accum(R + 12);
  for (;;) {
    const u32 t = __builtin_amdgcn_readfirstlane(tk);
    if (t >= ntile) break;
    tk = ticket();
    u32x4 D[4];
    WAITD();
    rd(D, slot);
    WAITL();
    dma4k(kp + toff(t), voff, ring + slot);
    score(D);
    WAITD();
    rd(D, slot + 4096);
    WAITL();
    dma4k(vp + toff(t), voff, ring + slot + 4096);
    accum(D);
    slot ^= 8192;
  }
  {
    u32x4 D[4];
    WAITV(12); rd(D, slot); score(D);
    WAITV(8); rd(D, slot + 4096); accum(D);
    WAITV(4); rd(D, slot ^ 8192); score(D);
    WAITV(0); rd(D, (slot ^ 8192) + 4096); accum(D);
  }
  l += dpp<XOR1>(l); l += dpp<XOR2>(l);
  {
    const float mw = xrow_max(m);
    const float sc = __builtin_amdgcn_exp2f(m - mw);
    l = xrow_sum(l * sc);
#pragma unroll
    for (int k = 0; k < 8; ++k) acc[k] = xrow_sum(acc[k] * sc);
    if (r == 0) {
      *reinterpret_cast<f32x4*>(lds + L_QP + sw * DH + c * 8) = f32x4{acc[0], acc[1], acc[2], acc[3]};
      *reinterpret_cast<f32x4*>(lds + L_QP + sw * DH + c * 8 + 4) = f32x4{acc[4], acc[5], acc[6], acc[7]};
      if (c == 0) { lds[L_MRG + sw * 2] = mw; lds[L_MRG + sw * 2 + 1] = l; }
    }
  }
  if (sw == 0) STAMP(9);
  if (sw == 7) STAMP(11);
  BAR();
}

extern "C" void kernel_launch(void* const* d_in, const int* in_sizes, int n_in, void* d_out, int out_size, void* d_ws, size_t ws_size,
                              hipStream_t stream) {
  const __hip_bfloat16* x = (const __hip_bfloat16*)d_in[0];
  const __hip_bfloat16* kc = (const __hip_bfloat16*)d_in[1];
  const __hip_bfloat16* vc = (const __hip_bfloat16*)d_in[2];
  const __hip_bfloat16* wq = (const __hip_bfloat16*)d_in[3];
  const __hip_bfloat16* wo = (const __hip_bfloat16*)d_in[4];
  const __hip_bfloat16* wg = (const __hip_bfloat16*)d_in[6];
  const __hip_bfloat16* wu = (const __hip_bfloat16*)d_in[7];
  const __hip_bfloat16* wd = (const __hip_bfloat16*)d_in[8];
  (void)hipFuncSetAttribute((const void*)fused_k, hipFuncAttributeMaxDynamicSharedMemorySize, L_END * 4);
  fused_k<<<NWG, NT, L_END * 4, stream>>>(x, kc, vc, wq, wo, wg, wu, wd, (char*)d_ws, (__hip_bfloat16*)d_out);
}
```

```cpp
#include <hip/hip_runtime.h>
#include <hip/hip_bf16.h>
#include <stdint.h>

typedef unsigned int u32;
typedef __attribute__((ext_vector_type(4))) unsigned int u32x4;
typedef __attribute__((ext_vector_type(4))) float f32x4;
typedef __attribute__((ext_vector_type(2))) float f32x2;
typedef __attribute__((ext_vector_type(2))) __bf16 bf16x2;

constexpr int NB = 2, DM = 512, NH = 32, SK = 16384, DH = 128, FF = 1376;
constexpr int NSP = 4;
constexpr int NW = 8;
constexpr int NT = (NW + 1) * 64;
constexpr int TOK_WG = SK / NSP;
constexpr int TILE = 16;
constexpr int NTILE = TOK_WG / (TILE * NW);
constexpr int NTWG = NTILE * NW;
constexpr int FSL = 32;
constexpr int NFS = FF / FSL;
constexpr int NWG = NB * NH * NSP;

__device__ __forceinline__ float bflo(u32 v) { return __builtin_bit_cast(float, v << 16); }
__device__ __forceinline__ float bfhi(u32 v) { return __builtin_bit_cast(float, v & 0xffff0000u); }
__device__ __forceinline__ u32 cvtpk(float lo, float hi) {
  u32 r; asm("v_cvt_pk_bf16_f32 %0, %1, %2" : "=v"(r) : "v"(lo), "v"(hi)); return r;
}
__device__ __forceinline__ float rbf(float x) { return bflo(cvtpk(x, x)); }
__device__ __forceinline__ float dot2(u32 k, u32 q, float acc) {
  return __builtin_amdgcn_fdot2_f32_bf16(__builtin_bit_cast(bf16x2, k), __builtin_bit_cast(bf16x2, q), acc, false);
}
template <int CTRL> __device__ __forceinline__ float dpp(float x) {
  return __builtin_bit_cast(float, __builtin_amdgcn_mov_dpp(__builtin_bit_cast(int, x), CTRL, 0xf, 0xf, true));
}
constexpr int XOR1 = 0xB1, XOR2 = 0x4E, XOR7 = 0x141, XOR8 = 0x128, ROR4 = 0x124;
__device__ __forceinline__ float xrow_sum(float x) {
  auto s = __builtin_amdgcn_permlane16_swap(__float_as_uint(x), __float_as_uint(x), false, false);
  x = __uint_as_float(s[0]) + __uint_as_float(s[1]);
  auto t = __builtin_amdgcn_permlane32_swap(__float_as_uint(x), __float_as_uint(x), false, false);
  return __uint_as_float(t[0]) + __uint_as_float(t[1]);
}
__device__ __forceinline__ float xrow_max(float x) {
  auto s = __builtin_amdgcn_permlane16_swap(__float_as_uint(x), __float_as_uint(x), false, false);
  x = fmaxf(__uint_as_float(s[0]), __uint_as_float(s[1]));
  auto t = __builtin_amdgcn_permlane32_swap(__float_as_uint(x), __float_as_uint(x), false, false);
  return fmaxf(__uint_as_float(t[0]), __uint_as_float(t[1]));
}
__device__ __forceinline__ float row16_sum(float x) {
  x += dpp<XOR1>(x); x += dpp<XOR2>(x); x += dpp<ROR4>(x); x += dpp<XOR8>(x); return x;
}
__device__ __forceinline__ float wave_sum(float x) { return xrow_sum(row16_sum(x)); }
__device__ __forceinline__ u32x4 ldnt(const void* p) { return __builtin_nontemporal_load(reinterpret_cast<const u32x4*>(p)); }
__device__ __forceinline__ u32x4 ld16(const void* p) { return *reinterpret_cast<const u32x4*>(p); }

typedef unsigned long long u64;
constexpr u32 W_QD = 0;
constexpr u32 W_O1 = 2u << 20;
constexpr u32 W_O2 = W_O1 + (256 << 10);
constexpr u32 W_Z = W_O1 + (512 << 10);
constexpr u32 W_CH = 3u << 20;
constexpr u32 W_R = W_CH + (32 << 10);
constexpr u32 W_LCG = W_R + (4 << 10);
constexpr u32 W_END = W_LCG + (4 << 10);
constexpr u32 SENT = 0xffffffffu;
constexpr int LEADER = 255;

typedef __attribute__((ext_vector_type(2))) unsigned int u32x2;
typedef __amdgpu_buffer_rsrc_t rsrc_t;
#define MKRS(p, bytes) __builtin_amdgcn_make_buffer_rsrc((void*)(p), 0, (int)(bytes), 0x00020000)
__device__ __forceinline__ u32x4 cl4(rsrc_t rs, u32 v, u32 so) { return __builtin_amdgcn_raw_buffer_load_b128(rs, (int)v, (int)so, 16); }
__device__ __forceinline__ u32x2 cl2(rsrc_t rs, u32 v, u32 so) { return __builtin_amdgcn_raw_buffer_load_b64(rs, (int)v, (int)so, 16); }
__device__ __forceinline__ u32 cl1(rsrc_t rs, u32 v, u32 so) { return __builtin_amdgcn_raw_buffer_load_b32(rs, (int)v, (int)so, 16); }
__device__ __forceinline__ void cs4(rsrc_t rs, u32 v, u32 so, u32x4 d) { __builtin_amdgcn_raw_buffer_store_b128(d, rs, (int)v, (int)so, 16); }
__device__ __forceinline__ void cs2(rsrc_t rs, u32 v, u32 so, u32x2 d) { __builtin_amdgcn_raw_buffer_store_b64(d, rs, (int)v, (int)so, 16); }
__device__ __forceinline__ void cs1(rsrc_t rs, u32 v, u32 so, u32 d) { __builtin_amdgcn_raw_buffer_store_b32(d, rs, (int)v, (int)so, 16); }
__device__ __forceinline__ u32x4 wl4(rsrc_t rs, u32 v, u32 so) { return __builtin_amdgcn_raw_buffer_load_b128(rs, (int)v, (int)so, 2); }
__device__ __forceinline__ u32 wl1(rsrc_t rs, u32 v, u32 so) { return __builtin_amdgcn_raw_buffer_load_b32(rs, (int)v, (int)so, 2); }
#define FENCE() asm volatile("" ::: "memory")
#define POLL() asm volatile("" ::: "memory")
__device__ __forceinline__ u32 canon(float f) { const u32 v = __float_as_uint(f); return v == SENT ? 0x7fffffffu : v; }
__device__ __forceinline__ float asf(u32 v) { return __uint_as_float(v); }
__device__ __forceinline__ bool valid4(u32x4 v) { return (v[0] != SENT) & (v[1] != SENT) & (v[2] != SENT) & (v[3] != SENT); }
__device__ __forceinline__ int wgid(int h, int j) { return (h >> 2) + 8 * (((h & 3) << 3) + j); }

#define STAMP(i) do {} while (0)
#define THR 1
constexpr int RING_W = 16384;
constexpr int L_XN = NW * RING_W / 4;
constexpr int L_QP = L_XN + 2 * DM;
constexpr int L_MRG = L_QP + NW * DH;
constexpr int L_QK = L_MRG + 16;
constexpr int L_OP = L_QK + 64;
constexpr int L_FP = L_OP + 128;
constexpr int L_HH = L_FP + 128;
constexpr int L_TK = L_HH + 64;
constexpr int L_END = L_TK + 4;

__device__ __forceinline__ float gelu_tanh(float x) {
  const float k0 = 0.7978845608028654f, k1 = 0.044715f;
  float u = k0 * (x + k1 * x * x * x);
  return 0.5f * x * (1.0f + tanhf(u));
}
__device__ __forceinline__ void dma4k(const void* sbase, u32 voff, u32 ldst) {
  u32 keep;
  asm volatile("s_mov_b32 %0, m0\n\ts_mov_b32 m0, %3\n\ts_nop 0\n\t"
               "global_load_lds_dwordx4 %1, %2 nt\n\t"
               "global_load_lds_dwordx4 %1, %2 offset:1024 nt\n\t"
               "global_load_lds_dwordx4 %1, %2 offset:2048 nt\n\t"
               "global_load_lds_dwordx4 %1, %2 offset:3072 nt\n\t"
               "s_mov_b32 m0, %0"
               : "=&s"(keep) : "v"(voff), "s"(sbase), "s"(ldst) : "memory");
}
__device__ __forceinline__ void touch(const void* sbase, u32 voff, u32 ldst) {
  u32 keep;
  asm volatile("s_mov_b32 %0, m0\n\ts_mov_b32 m0, %3\n\ts_nop 0\n\t"
               "global_load_lds_dword %1, %2 nt\n\t"
               "s_mov_b32 m0, %0"
               : "=&s"(keep) : "v"(voff), "s"(sbase), "s"(ldst) : "memory");
}
#define TSH 15
__device__ __forceinline__ void ld4k(u32x4* R, const void* sbase, u32 voff) {
  asm volatile("global_load_dwordx4 %0, %4, %5 nt\n\t"
               "global_load_dwordx4 %1, %4, %5 offset:1024 nt\n\t"
               "global_load_dwordx4 %2, %4, %5 offset:2048 nt\n\t"
               "global_load_dwordx4 %3, %4, %5 offset:3072 nt"
               : "=&v"(R[0]), "=&v"(R[1]), "=&v"(R[2]), "=&v"(R[3]) : "v"(voff), "s"(sbase) : "memory");
}
#define PIN4(n, a) asm volatile("s_waitcnt vmcnt(" #n ")" : "+v"((a)[0]), "+v"((a)[1]), "+v"((a)[2]), "+v"((a)[3]) :: "memory")
#define WAITV(n) asm volatile("s_waitcnt vmcnt(" #n ")" ::: "memory")
#define BK1 99
#define SL1 48
#define BK2 99
#define SL2 48
#define FD 7
#define DEPTH 4
#define WAITV2(n) WAITV(n)
#define WAITD() WAITV2(DEPTH)
#define WAITL() asm volatile("s_waitcnt lgkmcnt(0)" ::: "memory")
#define BAR() do { WAITL(); __builtin_amdgcn_s_barrier(); asm volatile("" ::: "memory"); } while (0)

__global__ void __launch_bounds__(NT) fused_k(const __hip_bfloat16* __restrict__ x, const __hip_bfloat16* __restrict__ kc,
                                               const __hip_bfloat16* __restrict__ vc, const __hip_bfloat16* __restrict__ wq,
                                               const __hip_bfloat16* __restrict__ wo, const __hip_bfloat16* __restrict__ wg,
                                               const __hip_bfloat16* __restrict__ wu, const __hip_bfloat16* __restrict__ wd,
                                               char* ws, __hip_bfloat16* __restrict__ out) {
  extern __shared__ __attribute__((aligned(16))) float lds[];
  const int tid = threadIdx.x, lane = tid & 63, wave = __builtin_amdgcn_readfirstlane(tid >> 6);
  const int r = lane >> 4, c = lane & 15;
  const int bid = blockIdx.x;
  const int xcd = bid & 7, j = (bid >> 3) & 7, hl = bid >> 6;
  const int h = xcd * 4 + hl, b = j >> 2, sp = j & 3;
  const int fe = (hl * 2 + b) * 8 + xcd;
  const bool fgrp = fe < NFS;
  constexpr float LOG2E = 1.4426950408889634f;

  if (wave == 0) {
    __builtin_amdgcn_s_setprio(3);
    STAMP(0);
    const rsrc_t rw = MKRS(ws, W_END);
    const u32 l16 = lane * 16, l8 = lane * 8;
    int spin = 0; (void)spin;
    const u32 inbox = W_QD + bid * 8192;
    const u32 T0 = (u32)(__builtin_amdgcn_s_memrealtime() >> 11);
    const u32 chdst = wgid(h, lane & 7) * 128;
    if (lane < 8 && lane != j) cs1(rw, chdst, W_CH + j * 4, T0);
    const u32x4 xr0 = ld16(x + lane * 8), xr1 = ld16(x + DM + lane * 8);
    u32x4 wqr[16];
    {
      const rsrc_t rq = MKRS(wq, DM * NH * DH * 2);
      const u32 vo = (r * 16 * (NH * DH) + c * 8) * 2;
#pragma unroll
      for (int i = 0; i < 16; ++i) wqr[i] = wl4(rq, vo, ((j * 64 + i) * (NH * DH) + h * DH) * 2);
    }
    u32x4 g[8];
#pragma unroll
    for (int jp = 0; jp < 8; ++jp) g[jp] = cl4(rw, l16, inbox + jp * 1024);
    const u32 g0v = cl1(rw, 0, W_LCG + bid * 8 + 4);
    u32 le[4] = {0, 0, 0, 0};
    if (bid == LEADER) {
#pragma unroll
      for (int k = 0; k < 4; ++k) le[k] = cl1(rw, l8, W_R + 512 * k);
    }
    if (lane == 0) reinterpret_cast<u32*>(lds)[L_TK + 1] = 0;
    BAR();
    STAMP(4);
#pragma unroll
    for (int bb = 0; bb < 2; ++bb) {
      const u32x4 xv = bb ? xr1 : xr0;
      float e[8];
#pragma unroll
      for (int k = 0; k < 4; ++k) { e[2 * k] = bflo(xv[k]); e[2 * k + 1] = bfhi(xv[k]); }
      float s = 0.f;
#pragma unroll
      for (int k = 0; k < 8; ++k) s += e[k];
      const float mean = wave_sum(s) * (1.0f / DM);
      float q2 = 0.f;
#pragma unroll
      for (int k = 0; k < 8; ++k) { const float d = e[k] - mean; q2 = fmaf(d, d, q2); }
      const float var = rbf(wave_sum(q2) * (1.0f / DM));
      const float mb = rbf(mean);
      const float den = rbf(sqrtf(rbf(var + 1e-5f)));
      float o[8];
#pragma unroll
      for (int k = 0; k < 8; ++k) o[k] = rbf(rbf(e[k] - mb) / den);
      *reinterpret_cast<f32x4*>(lds + L_XN + bb * DM + lane * 8) = f32x4{o[0], o[1], o[2], o[3]};
      *reinterpret_cast<f32x4*>(lds + L_XN + bb * DM + lane * 8 + 4) = f32x4{o[4], o[5], o[6], o[7]};
    }
    float v[2][2];
    {
      float a[2][8];
#pragma unroll
      for (int bb = 0; bb < 2; ++bb)
#pragma unroll
        for (int k = 0; k < 8; ++k) a[bb][k] = 0.f;
#pragma unroll
      for (int i4 = 0; i4 < 4; ++i4) {
        const f32x4 xa = *reinterpret_cast<const f32x4*>(lds + L_XN + j * 64 + r * 16 + i4 * 4);
        const f32x4 xb = *reinterpret_cast<const f32x4*>(lds + L_XN + DM + j * 64 + r * 16 + i4 * 4);
#pragma unroll
        for (int i = 0; i < 4; ++i)
#pragma unroll
          for (int k = 0; k < 4; ++k) {
            const float wl = bflo(wqr[i4 * 4 + i][k]), wh = bfhi(wqr[i4 * 4 + i][k]);
            a[0][2 * k] = fmaf(xa[i], wl, a[0][2 * k]); a[0][2 * k + 1] = fmaf(xa[i], wh, a[0][2 * k + 1]);
            a[1][2 * k] = fmaf(xb[i], wl, a[1][2 * k]); a[1][2 * k + 1] = fmaf(xb[i], wh, a[1][2 * k + 1]);
          }
      }
#pragma unroll
      for (int bb = 0; bb < 2; ++bb) {
        float t[4];
#pragma unroll
        for (int k = 0; k < 4; ++k) {
          auto sw = __builtin_amdgcn_permlane32_swap(__float_as_uint(a[bb][k]), __float_as_uint(a[bb][4 + k]), false, false);
          t[k] = asf(sw[0]) + asf(sw[1]);
        }
#pragma unroll
        for (int e = 0; e < 2; ++e) {
          auto sw = __builtin_amdgcn_permlane16_swap(__float_as_uint(t[e]), __float_as_uint(t[2 + e]), false, false);
          v[bb][e] = asf(sw[0]) + asf(sw[1]);
        }
      }
    }
    STAMP(5);
#pragma unroll
    for (int jc = 0; jc < 8; ++jc)
      if (jc != j) cs4(rw, l16, W_QD + (wgid(h, jc) * 8 + j) * 1024, u32x4{__float_as_uint(v[jc >> 2][0]), T0, __float_as_uint(v[jc >> 2][1]), T0});
    u32 Tlo = T0 - 1u, Tspan = 2u;
    for (;;) {
      bool hit = false;
#pragma unroll
      for (int jp = 0; jp < 8; ++jp) hit |= (g[jp][1] - Tlo <= Tspan) | (g[jp][3] - Tlo <= Tspan);
      if (__builtin_amdgcn_ballot_w64(hit) == 0) break;
      Tlo += Tspan + 1u; Tspan = 0u;
    }
    if (Tspan == 0u && lane < 8 && lane != j) cs1(rw, chdst, W_CH + j * 4, Tlo);
    const u32 gch = __builtin_amdgcn_readfirstlane(g0v) + 1u;
    u32 last[8];
#pragma unroll
    for (int k = 0; k < 8; ++k) last[k] = T0;
    const u32 chv_off = (lane & 7) * 4, chs_off = W_CH + bid * 128;
    auto service = [&](u32 chv) __attribute__((always_inline)) {
#pragma unroll
      for (int jc = 0; jc < 8; ++jc) {
        const u32 t = __builtin_amdgcn_readlane(chv, jc);
        if (jc != j && t != last[jc]) {
          last[jc] = t;
          cs4(rw, l16, W_QD + (wgid(h, jc) * 8 + j) * 1024, u32x4{__float_as_uint(v[jc >> 2][0]), t, __float_as_uint(v[jc >> 2][1]), t});
        }
      }
    };
    u32 miss = 0xffu & ~(1u << j);
    for (int it = 0;;) {
      POLL();
      const u32 chv = cl1(rw, chv_off, chs_off);
#pragma unroll
      for (int jp = 0; jp < 8; ++jp) if (miss >> jp & 1) g[jp] = cl4(rw, l16, inbox + jp * 1024);
#pragma unroll
      for (int jp = 0; jp < 8; ++jp)
        if ((miss >> jp & 1) && __builtin_amdgcn_ballot_w64((g[jp][1] - Tlo > Tspan) | (g[jp][3] - Tlo > Tspan)) == 0) miss &= ~(1u << jp);
      if (miss == 0) break;
      if (it >= 3) service(chv); else ++it;
    }
    {
      const float own0 = b ? v[1][0] : v[0][0], own1 = b ? v[1][1] : v[0][1];
      float q0 = 0.f, q1 = 0.f;
#pragma unroll
      for (int jp = 0; jp < 8; ++jp) { q0 += (jp == j) ? own0 : asf(g[jp][0]); q1 += (jp == j) ? own1 : asf(g[jp][2]); }
      reinterpret_cast<u32*>(lds)[L_QK + 4 * c + r] = cvtpk(q0, q1);
      if (lane == 0) reinterpret_cast<u32*>(lds)[L_TK] = 6 * NW;
    }
    WAITL();
    if (lane == 0) __hip_atomic_store(reinterpret_cast<u32*>(lds) + L_TK + 1, 1u, __ATOMIC_RELAXED, __HIP_MEMORY_SCOPE_WORKGROUP);
    FENCE();
    STAMP(1);
    __builtin_amdgcn_s_setprio(0);
    cs4(rw, l16, W_O1 + bid * 1024, u32x4{SENT, SENT, SENT, SENT});
    cs2(rw, l8, W_O2 + bid * 512, u32x2{SENT, SENT});
    if (fgrp && sp == 0) {
#pragma unroll
      for (int k = 0; k < 4; ++k) cs4(rw, l16, W_Z + fe * (2 * DM * 4) + k * 1024, u32x4{SENT, SENT, SENT, SENT});
    }
    if (bid == LEADER) {
#pragma unroll
      for (int k = 0; k < 4; ++k) { le[k] += 1u; cs1(rw, l8, W_LCG + 512 * k, le[k]); }
    }
    WAITV(0);
    {
      u32 lastlc = 0; bool f1 = true, ldone = false;
      for (;;) {
        POLL();
        const u32x2 lcg = cl2(rw, 0, W_LCG + bid * 8);
        const u32 chv = cl1(rw, chv_off, chs_off);
        u32x2 rr4[4];
        if (bid == LEADER) {
#pragma unroll
          for (int k = 0; k < 4; ++k) rr4[k] = cl2(rw, l8, W_R + 512 * k);
        }
        const u32 lc = __builtin_amdgcn_readfirstlane(lcg[0]), lg = __builtin_amdgcn_readfirstlane(lcg[1]);
        if (lg == gch) break;
        if (f1 || lc != lastlc) { if (lane == 0) cs2(rw, 0, W_R + bid * 8, u32x2{lc, gch}); lastlc = lc; f1 = false; }
        service(chv);
        if (bid == LEADER && !ldone) {
          bool bad = false;
#pragma unroll
          for (int k = 0; k < 4; ++k) bad |= rr4[k][0] != le[k];
          if (__builtin_amdgcn_ballot_w64(bad) == 0) {
#pragma unroll
            for (int k = 0; k < 4; ++k) cs1(rw, l8, W_LCG + 512 * k + 4, rr4[k][1]);
            ldone = true;
          }
        }
        __builtin_amdgcn_s_sleep(16);
      }
    }
    STAMP(6);
    if (fgrp && sp == 0) {
      const int f0 = fe * FSL, rr = lane >> 2, cc = lane & 3;
      float ag[2][8], au[2][8];
#pragma unroll
      for (int bb = 0; bb < 2; ++bb)
#pragma unroll
        for (int k = 0; k < 8; ++k) { ag[bb][k] = 0.f; au[bb][k] = 0.f; }
      const rsrc_t rg = MKRS(wg, DM * FF * 2), ru = MKRS(wu, DM * FF * 2), rd = MKRS(wd, DM * FF * 2);
      const u32 vo = (rr * 32 * FF + cc * 8) * 2;
#pragma unroll 1
      for (int ch = 0; ch < 4; ++ch) {
        u32x4 fg[8], fu[8];
#pragma unroll
        for (int i = 0; i < 8; ++i) { const u32 so = ((ch * 8 + i) * FF + f0) * 2; fg[i] = wl4(rg, vo, so); fu[i] = wl4(ru, vo, so); }
#pragma unroll
        for (int i4 = 0; i4 < 2; ++i4) {
          const f32x4 xa = *reinterpret_cast<const f32x4*>(lds + L_XN + rr * 32 + ch * 8 + i4 * 4);
          const f32x4 xb = *reinterpret_cast<const f32x4*>(lds + L_XN + DM + rr * 32 + ch * 8 + i4 * 4);
#pragma unroll
          for (int i = 0; i < 4; ++i)
#pragma unroll
            for (int k = 0; k < 4; ++k) {
              const float gl = bflo(fg[i4 * 4 + i][k]), gh = bfhi(fg[i4 * 4 + i][k]), ul = bflo(fu[i4 * 4 + i][k]), uh = bfhi(fu[i4 * 4 + i][k]);
              ag[0][2 * k] = fmaf(xa[i], gl, ag[0][2 * k]); ag[0][2 * k + 1] = fmaf(xa[i], gh, ag[0][2 * k + 1]);
              ag[1][2 * k] = fmaf(xb[i], gl, ag[1][2 * k]); ag[1][2 * k + 1] = fmaf(xb[i], gh, ag[1][2 * k + 1]);
              au[0][2 * k] = fmaf(xa[i], ul, au[0][2 * k]); au[0][2 * k + 1] = fmaf(xa[i], uh, au[0][2 * k + 1]);
              au[1][2 * k] = fmaf(xb[i], ul, au[1][2 * k]); au[1][2 * k + 1] = fmaf(xb[i], uh, au[1][2 * k + 1]);
            }
        }
      }
#pragma unroll
      for (int bb = 0; bb < 2; ++bb) {
#pragma unroll
        for (int k = 0; k < 8; ++k) {
          float t2 = ag[bb][k]; t2 += dpp<ROR4>(t2); t2 += dpp<XOR8>(t2); ag[bb][k] = xrow_sum(t2);
          float u = au[bb][k]; u += dpp<ROR4>(u); u += dpp<XOR8>(u); au[bb][k] = xrow_sum(u);
        }
        if (lane < 4) {
          float* pg = lds + L_FP + bb * 32 + lane * 8;
          float* pu = lds + L_FP + 64 + bb * 32 + lane * 8;
          *reinterpret_cast<f32x4*>(pg) = f32x4{ag[bb][0], ag[bb][1], ag[bb][2], ag[bb][3]};
          *reinterpret_cast<f32x4*>(pg + 4) = f32x4{ag[bb][4], ag[bb][5], ag[bb][6], ag[bb][7]};
          *reinterpret_cast<f32x4*>(pu) = f32x4{au[bb][0], au[bb][1], au[bb][2], au[bb][3]};
          *reinterpret_cast<f32x4*>(pu + 4) = f32x4{au[bb][4], au[bb][5], au[bb][6], au[bb][7]};
        }
      }
      WAITL();
      {
        const float gl = rbf(gelu_tanh(rbf(lds[L_FP + lane])));
        lds[L_HH + lane] = rbf(gl * rbf(lds[L_FP + 64 + lane]));
      }
      WAITL();
      float z[2][8];
#pragma unroll
      for (int bb = 0; bb < 2; ++bb)
#pragma unroll
        for (int k = 0; k < 8; ++k) z[bb][k] = 0.f;
#pragma unroll 1
      for (int ch = 0; ch < 2; ++ch) {
        u32x4 fd[16];
#pragma unroll
        for (int i = 0; i < 16; ++i) fd[i] = wl4(rd, l16, (f0 + ch * 16 + i) * (DM * 2));
#pragma unroll
        for (int i4 = 0; i4 < 4; ++i4) {
          const f32x4 ha = *reinterpret_cast<const f32x4*>(lds + L_HH + ch * 16 + i4 * 4);
          const f32x4 hb = *reinterpret_cast<const f32x4*>(lds + L_HH + 32 + ch * 16 + i4 * 4);
#pragma unroll
          for (int i = 0; i < 4; ++i)
#pragma unroll
            for (int k = 0; k < 4; ++k) {
              const float wl = bflo(fd[i4 * 4 + i][k]), wh = bfhi(fd[i4 * 4 + i][k]);
              z[0][2 * k] = fmaf(ha[i], wl, z[0][2 * k]); z[0][2 * k + 1] = fmaf(ha[i], wh, z[0][2 * k + 1]);
              z[1][2 * k] = fmaf(hb[i], wl, z[1][2 * k]); z[1][2 * k + 1] = fmaf(hb[i], wh, z[1][2 * k + 1]);
            }
        }
      }
#pragma unroll
      for (int bb = 0; bb < 2; ++bb)
#pragma unroll
        for (int k = 0; k < 2; ++k)
          cs4(rw, lane * 32, W_Z + (fe * 2 + bb) * (DM * 4) + k * 16,
              u32x4{canon(z[bb][4 * k]), canon(z[bb][4 * k + 1]), canon(z[bb][4 * k + 2]), canon(z[bb][4 * k + 3])});
    }
    STAMP(10);
    u32 P0[32], P1[32];
    {
      const rsrc_t ro = MKRS(wo, NH * DH * DM * 2);
      const u32 vo = ((lane >> 5) * 64 * DM + (lane & 31) * 2) * 2, so = (h * DH * DM + j * 64) * 2;
#pragma unroll
      for (int k = 0; k < 32; ++k) { P0[k] = wl1(ro, vo, so + (2 * k) * (DM * 2)); P1[k] = wl1(ro, vo, so + (2 * k + 1) * (DM * 2)); }
#pragma unroll
      for (int k = 0; k < 32; ++k) {
        const u32 A = P0[k], B = P1[k];
        P0[k] = __builtin_amdgcn_perm(B, A, 0x05040100u); P1[k] = __builtin_amdgcn_perm(B, A, 0x07060302u);
        asm volatile("" : "+v"(P0[k]), "+v"(P1[k]));
      }
    }
    const int oidx = (h >> 4) * DM + j * 64 + (h & 15) * 4;
    float gt[4], xo[4];
    {
      u32x4 zv = {0, 0, 0, 0};
      u64 want = (1ull << NFS) - 1;
      for (;;) {
        POLL();
        if (want >> lane & 1) zv = cl4(rw, lane * (2 * DM * 4), W_Z + oidx * 4);
        const u64 bad = __builtin_amdgcn_ballot_w64(!valid4(zv));
        if (bad == 0) break;
        if (bad & want) { want = bad & (0 - bad); __builtin_amdgcn_s_sleep(64); } else want = bad;
      }
      const u32x2 xv = *reinterpret_cast<const u32x2*>(x + oidx);
#pragma unroll
      for (int k = 0; k < 4; ++k) { const float f = rbf(wave_sum(asf(zv[k]))); gt[k] = rbf(f / (1.0f + __expf(-f))); }
      xo[0] = bflo(xv[0]); xo[1] = bfhi(xv[0]); xo[2] = bflo(xv[1]); xo[3] = bfhi(xv[1]);
    }
    STAMP(8);
    __builtin_amdgcn_s_setprio(3);
    BAR();
    float oM, oL, o0 = 0.f, o1 = 0.f;
    {
      f32x4 ml[4];
#pragma unroll
      for (int i = 0; i < 4; ++i) ml[i] = *reinterpret_cast<const f32x4*>(lds + L_MRG + i * 4);
      oM = fmaxf(fmaxf(fmaxf(ml[0][0], ml[0][2]), fmaxf(ml[1][0], ml[1][2])), fmaxf(fmaxf(ml[2][0], ml[2][2]), fmaxf(ml[3][0], ml[3][2])));
      oL = 0.f;
#pragma unroll
      for (int w = 0; w < NW; ++w) {
        const float wt = __builtin_amdgcn_exp2f(ml[w >> 1][(w & 1) * 2] - oM);
        oL = fmaf(wt, ml[w >> 1][(w & 1) * 2 + 1], oL);
        const f32x2 pv = *reinterpret_cast<const f32x2*>(lds + L_QP + w * DH + lane * 2);
        o0 = fmaf(wt, pv[0], o0); o1 = fmaf(wt, pv[1], o1);
      }
    }
    cs4(rw, l16, W_O1 + bid * 1024, u32x4{canon(o0), canon(o1), canon(oM), canon(oL)});
    STAMP(2);
    miss = 0xffu & ~(1u << j);
    for (;;) {
      POLL();
#pragma unroll
      for (int jp = 0; jp < 8; ++jp) if (miss >> jp & 1) g[jp] = cl4(rw, l16, W_O1 + wgid(h, jp) * 1024);
#pragma unroll
      for (int jp = 0; jp < 8; ++jp)
        if ((miss >> jp & 1) && __builtin_amdgcn_ballot_w64(!valid4(g[jp])) == 0) miss &= ~(1u << jp);
      if (miss == 0) break;
      if (__builtin_popcount(miss) >= BK1) __builtin_amdgcn_s_sleep(SL1);
    }
    STAMP(7);
#pragma unroll
    for (int bb = 0; bb < 2; ++bb) {
      float pm[4], pl[4], p0[4], p1[4];
#pragma unroll
      for (int s = 0; s < 4; ++s) {
        const int jp = bb * 4 + s; const bool own = jp == j;
        p0[s] = own ? o0 : asf(g[jp][0]); p1[s] = own ? o1 : asf(g[jp][1]); pm[s] = own ? oM : asf(g[jp][2]); pl[s] = own ? oL : asf(g[jp][3]);
      }
      const float Mb = fmaxf(fmaxf(pm[0], pm[1]), fmaxf(pm[2], pm[3]));
      float Lb = 0.f, a0 = 0.f, a1 = 0.f;
#pragma unroll
      for (int s = 0; s < 4; ++s) { const float wt = __builtin_amdgcn_exp2f(pm[s] - Mb); Lb = fmaf(wt, pl[s], Lb); a0 = fmaf(wt, p0[s], a0); a1 = fmaf(wt, p1[s], a1); }
      const float inv = 1.0f / Lb;
      reinterpret_cast<u32*>(lds)[L_OP + bb * 64 + lane] = cvtpk(a0 * inv, a1 * inv);
    }
    WAITL();
    float y[2][2] = {{0.f, 0.f}, {0.f, 0.f}};
#pragma unroll
    for (int k4 = 0; k4 < 8; ++k4)
#pragma unroll
      for (int bb = 0; bb < 2; ++bb) {
        const u32x4 ov = *reinterpret_cast<const u32x4*>(lds + L_OP + bb * 64 + (lane >> 5) * 32 + k4 * 4);
#pragma unroll
        for (int k = 0; k < 4; ++k) { y[bb][0] = dot2(P0[k4 * 4 + k], ov[k], y[bb][0]); y[bb][1] = dot2(P1[k4 * 4 + k], ov[k], y[bb][1]); }
      }
    {
      auto s0 = __builtin_amdgcn_permlane32_swap(__float_as_uint(y[0][0]), __float_as_uint(y[1][0]), false, false);
      auto s1 = __builtin_amdgcn_permlane32_swap(__float_as_uint(y[0][1]), __float_as_uint(y[1][1]), false, false);
      cs2(rw, l8, W_O2 + bid * 512, u32x2{canon(__uint_as_float(s0[0]) + __uint_as_float(s0[1])), canon(__uint_as_float(s1[0]) + __uint_as_float(s1[1]))});
    }
    u32x4 yv = {0, 0, 0, 0};
    {
      const u32 vo = wgid(lane & 31, j) * 512;
      bool need = lane < 32;
      for (;;) {
        POLL();
        if (need) { yv = cl4(rw, vo, W_O2 + h * 16); need = !valid4(yv); }
        const u64 bad = __builtin_amdgcn_ballot_w64(need);
        if (bad == 0) break;
        if (__builtin_popcountll(bad) >= BK2) __builtin_amdgcn_s_sleep(SL2);
      }
    }
    {
      float s[4] = {asf(yv[0]), asf(yv[1]), asf(yv[2]), asf(yv[3])};
#pragma unroll
      for (int k = 0; k < 4; ++k) {
        float t = row16_sum(s[k]);
        auto sw = __builtin_amdgcn_permlane16_swap(__float_as_uint(t), __float_as_uint(t), false, false);
        t = __uint_as_float(sw[0]) + __uint_as_float(sw[1]);
        s[k] = xo[k] + rbf(rbf(t) * gt[k]);
      }
      if (lane == 0) *reinterpret_cast<u32x2*>(out + oidx) = u32x2{cvtpk(s[0], s[1]), cvtpk(s[2], s[3])};
    }
    STAMP(3);
    return;
  }

  const int sw = wave - 1;
  const int t0 = fgrp ? (sp == 0 ? 0 : (256 - 3 * FD) + (256 + FD) * (sp - 1)) : 256 * sp;
  const u32 ntile = fgrp ? (sp == 0 ? 256u - 3 * FD : 256u + FD) : 256u;
  const size_t kvbase = ((size_t)(b * NH + h) * SK + (size_t)t0 * TILE) * DH;
  const char* kp = reinterpret_cast<const char*>(kc + kvbase);
  const char* vp = reinterpret_cast<const char*>(vc + kvbase);
  const u32 rot = (u32)((bid >> 3) * 8) % ntile;
  auto toff = [&](u32 t) -> size_t { u32 u = t + rot; if (u >= ntile) u -= ntile; return (size_t)u * 4096; };
  const u32 voff = lane * 16;
  const u32 ring = (u32)(uintptr_t)(__attribute__((address_space(3))) float*)lds + sw * RING_W;
  __builtin_amdgcn_s_barrier();
  u32x4 R[16];
  dma4k(kp + toff(sw), voff, ring);
  dma4k(vp + toff(sw), voff, ring + 4096);
  WAITV(4); dma4k(kp + toff((NW + sw)), voff, ring + 8192);
  WAITV(4); dma4k(vp + toff((NW + sw)), voff, ring + 12288);
  WAITV(4); ld4k(R, kp + toff((2 * NW + sw)), voff);
  WAITV(4); ld4k(R + 4, vp + toff((2 * NW + sw)), voff);
  WAITV(4); ld4k(R + 8, kp + toff((3 * NW + sw)), voff);
  WAITV(4); ld4k(R + 12, vp + toff((3 * NW + sw)), voff);
  if (sw < 2 && lane < (1 << (20 - TSH)))
    touch(sw ? vp : kp, (u32)lane << TSH, (u32)(uintptr_t)(__attribute__((address_space(3))) float*)lds + (L_QP + sw * DH) * 4);
  for (;;) {
    FENCE();
    if (__builtin_amdgcn_readfirstlane(__hip_atomic_load(reinterpret_cast<u32*>(lds) + L_TK + 1, __ATOMIC_RELAXED, __HIP_MEMORY_SCOPE_WORKGROUP)) != 0) break;
    __builtin_amdgcn_s_sleep(2);
  }
  FENCE();
  const u32x4 qpk = *reinterpret_cast<const u32x4*>(lds + L_QK + 4 * c);

  constexpr float SCALE = 0.08838834764831845f;
  float m = -INFINITY, l = 0.f, acc[8];
#pragma unroll
  for (int k = 0; k < 8; ++k) acc[k] = 0.f;
  const bool b0 = (c & 1) != 0, b1 = (c & 2) != 0;
  const char* lbase = reinterpret_cast<const char*>(lds) + sw * RING_W + lane * 16;
  float p0, p1, p2, p3;

  auto score = [&](const u32x4* K) __attribute__((always_inline)) {
    float d[4];
#pragma unroll
    for (int t = 0; t < 4; ++t) {
      float s = dot2(K[t][0], qpk[0], 0.f);
      s = dot2(K[t][1], qpk[1], s); s = dot2(K[t][2], qpk[2], s); s = dot2(K[t][3], qpk[3], s);
      d[t] = s;
    }
    float e0 = b0 ? d[1] : d[0], f0 = b0 ? d[0] : d[1];
    float e1 = b0 ? d[3] : d[2], f1 = b0 ? d[2] : d[3];
    e0 += dpp<XOR1>(f0); e1 += dpp<XOR1>(f1);
    float g0 = b1 ? e1 : e0, g1 = b1 ? e0 : e1;
    g0 += dpp<XOR2>(g1);
    g0 += dpp<ROR4>(g0); g0 += dpp<XOR8>(g0);
    const float sc = rbf(rbf(g0) * SCALE) * LOG2E;
    float tm = fmaxf(sc, dpp<XOR1>(sc)); tm = fmaxf(tm, dpp<XOR2>(tm));
    const float mn = fmaxf(m, tm);
    const float alpha = __builtin_amdgcn_exp2f(m - mn), p = __builtin_amdgcn_exp2f(sc - mn);
    m = mn;
    l = fmaf(l, alpha, p);
    p0 = dpp<0x00>(p); p1 = dpp<0x55>(p); p2 = dpp<0xAA>(p); p3 = dpp<0xFF>(p);
#pragma unroll
    for (int k = 0; k < 8; ++k) acc[k] *= alpha;
  };
  auto accum = [&](const u32x4* V) __attribute__((always_inline)) {
#pragma unroll
    for (int i = 0; i < 4; ++i) {
      const float pt = i == 0 ? p0 : i == 1 ? p1 : i == 2 ? p2 : p3;
#pragma unroll
      for (int k = 0; k < 4; ++k) { acc[2 * k] = fmaf(pt, bflo(V[i][k]), acc[2 * k]); acc[2 * k + 1] = fmaf(pt, bfhi(V[i][k]), acc[2 * k + 1]); }
    }
  };
  auto rd = [&](u32x4* D, u32 off) __attribute__((always_inline)) {
#pragma unroll
    for (int i = 0; i < 4; ++i) D[i] = *reinterpret_cast<const u32x4*>(lbase + off + i * 1024);
  };
  u32* tkp = reinterpret_cast<u32*>(lds) + L_TK;
  auto ticket = [&]() -> u32 { u32 t = 0; if (lane == 0) t = __hip_atomic_fetch_add(tkp, 1u, __ATOMIC_RELAXED, __HIP_MEMORY_SCOPE_WORKGROUP); return t; };

  u32 slot = 0;
  u32 tk = ticket();
#pragma unroll
  for (int s2 = 0; s2 < 2; ++s2) {
    u32x4 D[4];
    WAITV(28);
    rd(D, s2 * 8192);
    WAITL();
    dma4k(kp + toff(((4 + s2) * NW + sw)), voff, ring + s2 * 8192);
    score(D);
    WAITV(28);
    rd(D, s2 * 8192 + 4096);
    WAITL();
    dma4k(vp + toff(((4 + s2) * NW + sw)), voff, ring + s2 * 8192 + 4096);
    accum(D);
  }
  PIN4(28, R); score(R);
  PIN4(24, R + 4); accum(R + 4);
  PIN4(20, R + 8); score(R + 8);
  PIN4(16, R + 12); accum(R + 12);
  for (;;) {
    const u32 t = __builtin_amdgcn_readfirstlane(tk);
    if (t >= ntile) break;
    tk = ticket();
    u32x4 D[4];
    WAITD();
    rd(D, slot);
    WAITL();
    dma4k(kp + toff(t), voff, ring + slot);
    score(D);
    WAITD();
    rd(D, slot + 4096);
    WAITL();
    dma4k(vp + toff(t), voff, ring + slot + 4096);
    accum(D);
    slot ^= 8192;
  }
  {
    u32x4 D[4];
    WAITV(12); rd(D, slot); score(D);
    WAITV(8); rd(D, slot + 4096); accum(D);
    WAITV(4); rd(D, slot ^ 8192); score(D);
    WAITV(0); rd(D, (slot ^ 8192) + 4096); accum(D);
  }
  l += dpp<XOR1>(l); l += dpp<XOR2>(l);
  {
    const float mw = xrow_max(m);
    const float sc = __builtin_amdgcn_exp2f(m - mw);
    l = xrow_sum(l * sc);
#pragma unroll
    for (int k = 0; k < 8; ++k) acc[k] = xrow_sum(acc[k] * sc);
    if (r == 0) {
      *reinterpret_cast<f32x4*>(lds + L_QP + sw * DH + c * 8) = f32x4{acc[0], acc[1], acc[2], acc[3]};
      *reinterpret_cast<f32x4*>(lds + L_QP + sw * DH + c * 8 + 4) = f32x4{acc[4], acc[5], acc[6], acc[7]};
      if (c == 0) { lds[L_MRG + sw * 2] = mw; lds[L_MRG + sw * 2 + 1] = l; }
    }
  }
  if (sw == 0) STAMP(9);
  if (sw == 7) STAMP(11);
  BAR();
}

extern "C" void kernel_launch(void* const* d_in, const int* in_sizes, int n_in, void* d_out, int out_size, void* d_ws, size_t ws_size,
                              hipStream_t stream) {
  const __hip_bfloat16* x = (const __hip_bfloat16*)d_in[0];
  const __hip_bfloat16* kc = (const __hip_bfloat16*)d_in[1];
  const __hip_bfloat16* vc = (const __hip_bfloat16*)d_in[2];
  const __hip_bfloat16* wq = (const __hip_bfloat16*)d_in[3];
  const __hip_bfloat16* wo = (const __hip_bfloat16*)d_in[4];
  const __hip_bfloat16* wg = (const __hip_bfloat16*)d_in[6];
  const __hip_bfloat16* wu = (const __hip_bfloat16*)d_in[7];
  const __hip_bfloat16* wd = (const __hip_bfloat16*)d_in[8];
  (void)hipFuncSetAttribute((const void*)fused_k, hipFuncAttributeMaxDynamicSharedMemorySize, L_END * 4);
  fused_k<<<NWG, NT, L_END * 4, stream>>>(x, kc, vc, wq, wo, wg, wu, wd, (char*)d_ws, (__hip_bfloat16*)d_out);
}
```

```cpp
#include <hip/hip_runtime.h>
#include <hip/hip_bf16.h>
#include <stdint.h>

typedef unsigned int u32;
typedef __attribute__((ext_vector_type(4))) unsigned int u32x4;
typedef __attribute__((ext_vector_type(4))) float f32x4;
typedef __attribute__((ext_vector_type(2))) float f32x2;
typedef __attribute__((ext_vector_type(2))) __bf16 bf16x2;

constexpr int NB = 2, DM = 512, NH = 32, SK = 16384, DH = 128, FF = 1376;
constexpr int NSP = 4;
constexpr int NW = 8;
constexpr int NT = (NW + 1) * 64;
constexpr int TOK_WG = SK / NSP;
constexpr int TILE = 16;
constexpr int NTILE = TOK_WG / (TILE * NW);
constexpr int NTWG = NTILE * NW;
constexpr int FSL = 32;
constexpr int NFS = FF / FSL;
constexpr int NWG = NB * NH * NSP;

__device__ __forceinline__ float bflo(u32 v) { return __builtin_bit_cast(float, v << 16); }
__device__ __forceinline__ float bfhi(u32 v) { return __builtin_bit_cast(float, v & 0xffff0000u); }
__device__ __forceinline__ u32 cvtpk(float lo, float hi) {
  u32 r; asm("v_cvt_pk_bf16_f32 %0, %1, %2" : "=v"(r) : "v"(lo), "v"(hi)); return r;
}
__device__ __forceinline__ float rbf(float x) { return bflo(cvtpk(x, x)); }
__device__ __forceinline__ float dot2(u32 k, u32 q, float acc) {
  return __builtin_amdgcn_fdot2_f32_bf16(__builtin_bit_cast(bf16x2, k), __builtin_bit_cast(bf16x2, q), acc, false);
}
template <int CTRL> __device__ __forceinline__ float dpp(float x) {
  return __builtin_bit_cast(float, __builtin_amdgcn_mov_dpp(__builtin_bit_cast(int, x), CTRL, 0xf, 0xf, true));
}
constexpr int XOR1 = 0xB1, XOR2 = 0x4E, XOR7 = 0x141, XOR8 = 0x128, ROR4 = 0x124;
__device__ __forceinline__ float xrow_sum(float x) {
  auto s = __builtin_amdgcn_permlane16_swap(__float_as_uint(x), __float_as_uint(x), false, false);
  x = __uint_as_float(s[0]) + __uint_as_float(s[1]);
  auto t = __builtin_amdgcn_permlane32_swap(__float_as_uint(x), __float_as_uint(x), false, false);
  return __uint_as_float(t[0]) + __uint_as_float(t[1]);
}
__device__ __forceinline__ float xrow_max(float x) {
  auto s = __builtin_amdgcn_permlane16_swap(__float_as_uint(x), __float_as_uint(x), false, false);
  x = fmaxf(__uint_as_float(s[0]), __uint_as_float(s[1]));
  auto t = __builtin_amdgcn_permlane32_swap(__float_as_uint(x), __float_as_uint(x), false, false);
  return fmaxf(__uint_as_float(t[0]), __uint_as_float(t[1]));
}
__device__ __forceinline__ float row16_sum(float x) {
  x += dpp<XOR1>(x); x += dpp<XOR2>(x); x += dpp<ROR4>(x); x += dpp<XOR8>(x); return x;
}
__device__ __forceinline__ float wave_sum(float x) { return xrow_sum(row16_sum(x)); }
__device__ __forceinline__ u32x4 ldnt(const void* p) { return __builtin_nontemporal_load(reinterpret_cast<const u32x4*>(p)); }
__device__ __forceinline__ u32x4 ld16(const void* p) { return *reinterpret_cast<const u32x4*>(p); }

typedef unsigned long long u64;
constexpr u32 W_QD = 0;
constexpr u32 W_O1 = 2u << 20;
constexpr u32 W_O2 = W_O1 + (256 << 10);
constexpr u32 W_Z = W_O1 + (512 << 10);
constexpr u32 W_CH = 3u << 20;
constexpr u32 W_R = W_CH + (32 << 10);
constexpr u32 W_LCG = W_R + (4 << 10);
constexpr u32 W_END = W_LCG + (4 << 10);
constexpr u32 SENT = 0xffffffffu;
constexpr int LEADER = 255;

typedef __attribute__((ext_vector_type(2))) unsigned int u32x2;
typedef __amdgpu_buffer_rsrc_t rsrc_t;
#define MKRS(p, bytes) __builtin_amdgcn_make_buffer_rsrc((void*)(p), 0, (int)(bytes), 0x00020000)
__device__ __forceinline__ u32x4 cl4(rsrc_t rs, u32 v, u32 so) { return __builtin_amdgcn_raw_buffer_load_b128(rs, (int)v, (int)so, 16); }
__device__ __forceinline__ u32x2 cl2(rsrc_t rs, u32 v, u32 so) { return __builtin_amdgcn_raw_buffer_load_b64(rs, (int)v, (int)so, 16); }
__device__ __forceinline__ u32 cl1(rsrc_t rs, u32 v, u32 so) { return __builtin_amdgcn_raw_buffer_load_b32(rs, (int)v, (int)so, 16); }
__device__ __forceinline__ void cs4(rsrc_t rs, u32 v, u32 so, u32x4 d) { __builtin_amdgcn_raw_buffer_store_b128(d, rs, (int)v, (int)so, 16); }
__device__ __forceinline__ void cs2(rsrc_t rs, u32 v, u32 so, u32x2 d) { __builtin_amdgcn_raw_buffer_store_b64(d, rs, (int)v, (int)so, 16); }
__device__ __forceinline__ void cs1(rsrc_t rs, u32 v, u32 so, u32 d) { __builtin_amdgcn_raw_buffer_store_b32(d, rs, (int)v, (int)so, 16); }
__device__ __forceinline__ u32x4 wl4(rsrc_t rs, u32 v, u32 so) { return __builtin_amdgcn_raw_buffer_load_b128(rs, (int)v, (int)so, 2); }
__device__ __forceinline__ u32 wl1(rsrc_t rs, u32 v, u32 so) { return __builtin_amdgcn_raw_buffer_load_b32(rs, (int)v, (int)so, 2); }
#define FENCE() asm volatile("" ::: "memory")
#define POLL() asm volatile("" ::: "memory")
__device__ __forceinline__ u32 canon(float f) { const u32 v = __float_as_uint(f); return v == SENT ? 0x7fffffffu : v; }
__device__ __forceinline__ float asf(u32 v) { return __uint_as_float(v); }
__device__ __forceinline__ bool valid4(u32x4 v) { return (v[0] != SENT) & (v[1] != SENT) & (v[2] != SENT) & (v[3] != SENT); }
__device__ __forceinline__ int wgid(int h, int j) { return (h >> 2) + 8 * (((h & 3) << 3) + j); }

#define STAMP(i) do {} while (0)
#define THR 1
constexpr int RING_W = 16384;
constexpr int L_XN = NW * RING_W / 4;
constexpr int L_QP = L_XN + 2 * DM;
constexpr int L_MRG = L_QP + NW * DH;
constexpr int L_QK = L_MRG + 16;
constexpr int L_OP = L_QK + 64;
constexpr int L_FP = L_OP + 128;
constexpr int L_HH = L_FP + 128;
constexpr int L_TK = L_HH + 64;
constexpr int L_END = L_TK + 4;

__device__ __forceinline__ float gelu_tanh(float x) {
  const float k0 = 0.7978845608028654f, k1 = 0.044715f;
  float u = k0 * (x + k1 * x * x * x);
  return 0.5f * x * (1.0f + tanhf(u));
}
__device__ __forceinline__ void dma4k(const void* sbase, u32 voff, u32 ldst) {
  u32 keep;
  asm volatile("s_mov_b32 %0, m0\n\ts_mov_b32 m0, %3\n\ts_nop 0\n\t"
               "global_load_lds_dwordx4 %1, %2 nt\n\t"
               "global_load_lds_dwordx4 %1, %2 offset:1024 nt\n\t"
               "global_load_lds_dwordx4 %1, %2 offset:2048 nt\n\t"
               "global_load_lds_dwordx4 %1, %2 offset:3072 nt\n\t"
               "s_mov_b32 m0, %0"
               : "=&s"(keep) : "v"(voff), "s"(sbase), "s"(ldst) : "memory");
}
__device__ __forceinline__ void ld4k(u32x4* R, const void* sbase, u32 voff) {
  asm volatile("global_load_dwordx4 %0, %4, %5 nt\n\t"
               "global_load_dwordx4 %1, %4, %5 offset:1024 nt\n\t"
               "global_load_dwordx4 %2, %4, %5 offset:2048 nt\n\t"
               "global_load_dwordx4 %3, %4, %5 offset:3072 nt"
               : "=&v"(R[0]), "=&v"(R[1]), "=&v"(R[2]), "=&v"(R[3]) : "v"(voff), "s"(sbase) : "memory");
}
#define PIN4(n, a) asm volatile("s_waitcnt vmcnt(" #n ")" : "+v"((a)[0]), "+v"((a)[1]), "+v"((a)[2]), "+v"((a)[3]) :: "memory")
#define WAITV(n) asm volatile("s_waitcnt vmcnt(" #n ")" ::: "memory")
#define BK1 99
#define SL1 48
#define BK2 99
#define SL2 48
#define FD 7
#define DEPTH 4
#define WAITV2(n) WAITV(n)
#define WAITD() WAITV2(DEPTH)
#define WAITL() asm volatile("s_waitcnt lgkmcnt(0)" ::: "memory")
#define BAR() do { WAITL(); __builtin_amdgcn_s_barrier(); asm volatile("" ::: "memory"); } while (0)

__global__ void __launch_bounds__(NT) fused_k(const __hip_bfloat16* __restrict__ x, const __hip_bfloat16* __restrict__ kc,
                                               const __hip_bfloat16* __restrict__ vc, const __hip_bfloat16* __restrict__ wq,
                                               const __hip_bfloat16* __restrict__ wo, const __hip_bfloat16* __restrict__ wg,
                                               const __hip_bfloat16* __restrict__ wu, const __hip_bfloat16* __restrict__ wd,
                                               char* ws, __hip_bfloat16* __restrict__ out) {
  extern __shared__ __attribute__((aligned(16))) float lds[];
  const int tid = threadIdx.x, lane = tid & 63, wave = __builtin_amdgcn_readfirstlane(tid >> 6);
  const int r = lane >> 4, c = lane & 15;
  const int bid = blockIdx.x;
  const int xcd = bid & 7, j = (bid >> 3) & 7, hl = bid >> 6;
  const int h = xcd * 4 + hl, b = j >> 2, sp = j & 3;
  const int fe = (hl * 2 + b) * 8 + xcd;
  const bool fgrp = fe < NFS;
  constexpr float LOG2E = 1.4426950408889634f;

  if (wave == 0) {
    __builtin_amdgcn_s_setprio(3);
    STAMP(0);
    const rsrc_t rw = MKRS(ws, W_END);
    const u32 l16 = lane * 16, l8 = lane * 8;
    int spin = 0; (void)spin;
    const u32 inbox = W_QD + bid * 8192;
    const u32 T0 = (u32)(__builtin_amdgcn_s_memrealtime() >> 11);
    const u32 chdst = wgid(h, lane & 7) * 128;
    if (lane < 8 && lane != j) cs1(rw, chdst, W_CH + j * 4, T0);
    const u32x4 xr0 = ld16(x + lane * 8), xr1 = ld16(x + DM + lane * 8);
    u32x4 wqr[16];
    {
      const rsrc_t rq = MKRS(wq, DM * NH * DH * 2);
      const u32 vo = (r * 16 * (NH * DH) + c * 8) * 2;
#pragma unroll
      for (int i = 0; i < 16; ++i) wqr[i] = wl4(rq, vo, ((j * 64 + i) * (NH * DH) + h * DH) * 2);
    }
    u32x4 g[8];
#pragma unroll
    for (int jp = 0; jp < 8; ++jp) g[jp] = cl4(rw, l16, inbox + jp * 1024);
    const u32 g0v = cl1(rw, 0, W_LCG + bid * 8 + 4);
    u32 le[4] = {0, 0, 0, 0};
    if (bid == LEADER) {
#pragma unroll
      for (int k = 0; k < 4; ++k) le[k] = cl1(rw, l8, W_R + 512 * k);
    }
    if (lane == 0) reinterpret_cast<u32*>(lds)[L_TK + 1] = 0;
    BAR();
    STAMP(4);
#pragma unroll
    for (int bb = 0; bb < 2; ++bb) {
      const u32x4 xv = bb ? xr1 : xr0;
      float e[8];
#pragma unroll
      for (int k = 0; k < 4; ++k) { e[2 * k] = bflo(xv[k]); e[2 * k + 1] = bfhi(xv[k]); }
      float s = 0.f;
#pragma unroll
      for (int k = 0; k < 8; ++k) s += e[k];
      const float mean = wave_sum(s) * (1.0f / DM);
      float q2 = 0.f;
#pragma unroll
      for (int k = 0; k < 8; ++k) { const float d = e[k] - mean; q2 = fmaf(d, d, q2); }
      const float var = rbf(wave_sum(q2) * (1.0f / DM));
      const float mb = rbf(mean);
      const float den = rbf(sqrtf(rbf(var + 1e-5f)));
      float o[8];
#pragma unroll
      for (int k = 0; k < 8; ++k) o[k] = rbf(rbf(e[k] - mb) / den);
      *reinterpret_cast<f32x4*>(lds + L_XN + bb * DM + lane * 8) = f32x4{o[0], o[1], o[2], o[3]};
      *reinterpret_cast<f32x4*>(lds + L_XN + bb * DM + lane * 8 + 4) = f32x4{o[4], o[5], o[6], o[7]};
    }
    float v[2][2];
    {
      float a[2][8];
#pragma unroll
      for (int bb = 0; bb < 2; ++bb)
#pragma unroll
        for (int k = 0; k < 8; ++k) a[bb][k] = 0.f;
#pragma unroll
      for (int i4 = 0; i4 < 4; ++i4) {
        const f32x4 xa = *reinterpret_cast<const f32x4*>(lds + L_XN + j * 64 + r * 16 + i4 * 4);
        const f32x4 xb = *reinterpret_cast<const f32x4*>(lds + L_XN + DM + j * 64 + r * 16 + i4 * 4);
#pragma unroll
        for (int i = 0; i < 4; ++i)
#pragma unroll
          for (int k = 0; k < 4; ++k) {
            const float wl = bflo(wqr[i4 * 4 + i][k]), wh = bfhi(wqr[i4 * 4 + i][k]);
            a[0][2 * k] = fmaf(xa[i], wl, a[0][2 * k]); a[0][2 * k + 1] = fmaf(xa[i], wh, a[0][2 * k + 1]);
            a[1][2 * k] = fmaf(xb[i], wl, a[1][2 * k]); a[1][2 * k + 1] = fmaf(xb[i], wh, a[1][2 * k + 1]);
          }
      }
#pragma unroll
      for (int bb = 0; bb < 2; ++bb) {
        float t[4];
#pragma unroll
        for (int k = 0; k < 4; ++k) {
          auto sw = __builtin_amdgcn_permlane32_swap(__float_as_uint(a[bb][k]), __float_as_uint(a[bb][4 + k]), false, false);
          t[k] = asf(sw[0]) + asf(sw[1]);
        }
#pragma unroll
        for (int e = 0; e < 2; ++e) {
          auto sw = __builtin_amdgcn_permlane16_swap(__float_as_uint(t[e]), __float_as_uint(t[2 + e]), false, false);
          v[bb][e] = asf(sw[0]) + asf(sw[1]);
        }
      }
    }
    STAMP(5);
#pragma unroll
    for (int jc = 0; jc < 8; ++jc)
      if (jc != j) cs4(rw, l16, W_QD + (wgid(h, jc) * 8 + j) * 1024, u32x4{__float_as_uint(v[jc >> 2][0]), T0, __float_as_uint(v[jc >> 2][1]), T0});
    u32 Tlo = T0 - 1u, Tspan = 2u;
    for (;;) {
      bool hit = false;
#pragma unroll
      for (int jp = 0; jp < 8; ++jp) hit |= (g[jp][1] - Tlo <= Tspan) | (g[jp][3] - Tlo <= Tspan);
      if (__builtin_amdgcn_ballot_w64(hit) == 0) break;
      Tlo += Tspan + 1u; Tspan = 0u;
    }
    if (Tspan == 0u && lane < 8 && lane != j) cs1(rw, chdst, W_CH + j * 4, Tlo);
    const u32 gch = __builtin_amdgcn_readfirstlane(g0v) + 1u;
    u32 last[8];
#pragma unroll
    for (int k = 0; k < 8; ++k) last[k] = T0;
    const u32 chv_off = (lane & 7) * 4, chs_off = W_CH + bid * 128;
    auto service = [&](u32 chv) __attribute__((always_inline)) {
#pragma unroll
      for (int jc = 0; jc < 8; ++jc) {
        const u32 t = __builtin_amdgcn_readlane(chv, jc);
        if (jc != j && t != last[jc]) {
          last[jc] = t;
          cs4(rw, l16, W_QD + (wgid(h, jc) * 8 + j) * 1024, u32x4{__float_as_uint(v[jc >> 2][0]), t, __float_as_uint(v[jc >> 2][1]), t});
        }
      }
    };
    u32 miss = 0xffu & ~(1u << j);
    for (int it = 0;;) {
      POLL();
      const u32 chv = cl1(rw, chv_off, chs_off);
#pragma unroll
      for (int jp = 0; jp < 8; ++jp) if (miss >> jp & 1) g[jp] = cl4(rw, l16, inbox + jp * 1024);
#pragma unroll
      for (int jp = 0; jp < 8; ++jp)
        if ((miss >> jp & 1) && __builtin_amdgcn_ballot_w64((g[jp][1] - Tlo > Tspan) | (g[jp][3] - Tlo > Tspan)) == 0) miss &= ~(1u << jp);
      if (miss == 0) break;
      if (it >= 3) service(chv); else ++it;
    }
    {
      const float own0 = b ? v[1][0] : v[0][0], own1 = b ? v[1][1] : v[0][1];
      float q0 = 0.f, q1 = 0.f;
#pragma unroll
      for (int jp = 0; jp < 8; ++jp) { q0 += (jp == j) ? own0 : asf(g[jp][0]); q1 += (jp == j) ? own1 : asf(g[jp][2]); }
      reinterpret_cast<u32*>(lds)[L_QK + 4 * c + r] = cvtpk(q0, q1);
      if (lane == 0) reinterpret_cast<u32*>(lds)[L_TK] = 6 * NW;
    }
    WAITL();
    if (lane == 0) __hip_atomic_store(reinterpret_cast<u32*>(lds) + L_TK + 1, 1u, __ATOMIC_RELAXED, __HIP_MEMORY_SCOPE_WORKGROUP);
    FENCE();
    STAMP(1);
    __builtin_amdgcn_s_setprio(0);
    cs4(rw, l16, W_O1 + bid * 1024, u32x4{SENT, SENT, SENT, SENT});
    cs2(rw, l8, W_O2 + bid * 512, u32x2{SENT, SENT});
    if (fgrp && sp == 0) {
#pragma unroll
      for (int k = 0; k < 4; ++k) cs4(rw, l16, W_Z + fe * (2 * DM * 4) + k * 1024, u32x4{SENT, SENT, SENT, SENT});
    }
    if (bid == LEADER) {
#pragma unroll
      for (int k = 0; k < 4; ++k) { le[k] += 1u; cs1(rw, l8, W_LCG + 512 * k, le[k]); }
    }
    WAITV(0);
    {
      u32 lastlc = 0; bool f1 = true, ldone = false;
      for (;;) {
        POLL();
        const u32x2 lcg = cl2(rw, 0, W_LCG + bid * 8);
        const u32 chv = cl1(rw, chv_off, chs_off);
        u32x2 rr4[4];
        if (bid == LEADER) {
#pragma unroll
          for (int k = 0; k < 4; ++k) rr4[k] = cl2(rw, l8, W_R + 512 * k);
        }
        const u32 lc = __builtin_amdgcn_readfirstlane(lcg[0]), lg = __builtin_amdgcn_readfirstlane(lcg[1]);
        if (lg == gch) break;
        if (f1 || lc != lastlc) { if (lane == 0) cs2(rw, 0, W_R + bid * 8, u32x2{lc, gch}); lastlc = lc; f1 = false; }
        service(chv);
        if (bid == LEADER && !ldone) {
          bool bad = false;
#pragma unroll
          for (int k = 0; k < 4; ++k) bad |= rr4[k][0] != le[k];
          if (__builtin_amdgcn_ballot_w64(bad) == 0) {
#pragma unroll
            for (int k = 0; k < 4; ++k) cs1(rw, l8, W_LCG + 512 * k + 4, rr4[k][1]);
            ldone = true;
          }
        }
        __builtin_amdgcn_s_sleep(16);
      }
    }
    STAMP(6);
    if (fgrp && sp == 0) {
      const int f0 = fe * FSL, rr = lane >> 2, cc = lane & 3;
      float ag[2][8], au[2][8];
#pragma unroll
      for (int bb = 0; bb < 2; ++bb)
#pragma unroll
        for (int k = 0; k < 8; ++k) { ag[bb][k] = 0.f; au[bb][k] = 0.f; }
      const rsrc_t rg = MKRS(wg, DM * FF * 2), ru = MKRS(wu, DM * FF * 2), rd = MKRS(wd, DM * FF * 2);
      const u32 vo = (rr * 32 * FF + cc * 8) * 2;
#pragma unroll 1
      for (int ch = 0; ch < 4; ++ch) {
        u32x4 fg[8], fu[8];
#pragma unroll
        for (int i = 0; i < 8; ++i) { const u32 so = ((ch * 8 + i) * FF + f0) * 2; fg[i] = wl4(rg, vo, so); fu[i] = wl4(ru, vo, so); }
#pragma unroll
        for (int i4 = 0; i4 < 2; ++i4) {
          const f32x4 xa = *reinterpret_cast<const f32x4*>(lds + L_XN + rr * 32 + ch * 8 + i4 * 4);
          const f32x4 xb = *reinterpret_cast<const f32x4*>(lds + L_XN + DM + rr * 32 + ch * 8 + i4 * 4);
#pragma unroll
          for (int i = 0; i < 4; ++i)
#pragma unroll
            for (int k = 0; k < 4; ++k) {
              const float gl = bflo(fg[i4 * 4 + i][k]), gh = bfhi(fg[i4 * 4 + i][k]), ul = bflo(fu[i4 * 4 + i][k]), uh = bfhi(fu[i4 * 4 + i][k]);
              ag[0][2 * k] = fmaf(xa[i], gl, ag[0][2 * k]); ag[0][2 * k + 1] = fmaf(xa[i], gh, ag[0][2 * k + 1]);
              ag[1][2 * k] = fmaf(xb[i], gl, ag[1][2 * k]); ag[1][2 * k + 1] = fmaf(xb[i], gh, ag[1][2 * k + 1]);
              au[0][2 * k] = fmaf(xa[i], ul, au[0][2 * k]); au[0][2 * k + 1] = fmaf(xa[i], uh, au[0][2 * k + 1]);
              au[1][2 * k] = fmaf(xb[i], ul, au[1][2 * k]); au[1][2 * k + 1] = fmaf(xb[i], uh, au[1][2 * k + 1]);
            }
        }
      }
#pragma unroll
      for (int bb = 0; bb < 2; ++bb) {
#pragma unroll
        for (int k = 0; k < 8; ++k) {
          float t2 = ag[bb][k]; t2 += dpp<ROR4>(t2); t2 += dpp<XOR8>(t2); ag[bb][k] = xrow_sum(t2);
          float u = au[bb][k]; u += dpp<ROR4>(u); u += dpp<XOR8>(u); au[bb][k] = xrow_sum(u);
        }
        if (lane < 4) {
          float* pg = lds + L_FP + bb * 32 + lane * 8;
          float* pu = lds + L_FP + 64 + bb * 32 + lane * 8;
          *reinterpret_cast<f32x4*>(pg) = f32x4{ag[bb][0], ag[bb][1], ag[bb][2], ag[bb][3]};
          *reinterpret_cast<f32x4*>(pg + 4) = f32x4{ag[bb][4], ag[bb][5], ag[bb][6], ag[bb][7]};
          *reinterpret_cast<f32x4*>(pu) = f32x4{au[bb][0], au[bb][1], au[bb][2], au[bb][3]};
          *reinterpret_cast<f32x4*>(pu + 4) = f32x4{au[bb][4], au[bb][5], au[bb][6], au[bb][7]};
        }
      }
      WAITL();
      {
        const float gl = rbf(gelu_tanh(rbf(lds[L_FP + lane])));
        lds[L_HH + lane] = rbf(gl * rbf(lds[L_FP + 64 + lane]));
      }
      WAITL();
      float z[2][8];
#pragma unroll
      for (int bb = 0; bb < 2; ++bb)
#pragma unroll
        for (int k = 0; k < 8; ++k) z[bb][k] = 0.f;
#pragma unroll 1
      for (int ch = 0; ch < 2; ++ch) {
        u32x4 fd[16];
#pragma unroll
        for (int i = 0; i < 16; ++i) fd[i] = wl4(rd, l16, (f0 + ch * 16 + i) * (DM * 2));
#pragma unroll
        for (int i4 = 0; i4 < 4; ++i4) {
          const f32x4 ha = *reinterpret_cast<const f32x4*>(lds + L_HH + ch * 16 + i4 * 4);
          const f32x4 hb = *reinterpret_cast<const f32x4*>(lds + L_HH + 32 + ch * 16 + i4 * 4);
#pragma unroll
          for (int i = 0; i < 4; ++i)
#pragma unroll
            for (int k = 0; k < 4; ++k) {
              const float wl = bflo(fd[i4 * 4 + i][k]), wh = bfhi(fd[i4 * 4 + i][k]);
              z[0][2 * k] = fmaf(ha[i], wl, z[0][2 * k]); z[0][2 * k + 1] = fmaf(ha[i], wh, z[0][2 * k + 1]);
              z[1][2 * k] = fmaf(hb[i], wl, z[1][2 * k]); z[1][2 * k + 1] = fmaf(hb[i], wh, z[1][2 * k + 1]);
            }
        }
      }
#pragma unroll
      for (int bb = 0; bb < 2; ++bb)
#pragma unroll
        for (int k = 0; k < 2; ++k)
          cs4(rw, lane * 32, W_Z + (fe * 2 + bb) * (DM * 4) + k * 16,
              u32x4{canon(z[bb][4 * k]), canon(z[bb][4 * k + 1]), canon(z[bb][4 * k + 2]), canon(z[bb][4 * k + 3])});
    }
    STAMP(10);
    u32 P0[32], P1[32];
    {
      const rsrc_t ro = MKRS(wo, NH * DH * DM * 2);
      const u32 vo = ((lane >> 5) * 64 * DM + (lane & 31) * 2) * 2, so = (h * DH * DM + j * 64) * 2;
#pragma unroll
      for (int k = 0; k < 32; ++k) { P0[k] = wl1(ro, vo, so + (2 * k) * (DM * 2)); P1[k] = wl1(ro, vo, so + (2 * k + 1) * (DM * 2)); }
#pragma unroll
      for (int k = 0; k < 32; ++k) {
        const u32 A = P0[k], B = P1[k];
        P0[k] = __builtin_amdgcn_perm(B, A, 0x05040100u); P1[k] = __builtin_amdgcn_perm(B, A, 0x07060302u);
        asm volatile("" : "+v"(P0[k]), "+v"(P1[k]));
      }
    }
    const int oidx = (h >> 4) * DM + j * 64 + (h & 15) * 4;
    float gt[4], xo[4];
    {
      u32x4 zv = {0, 0, 0, 0};
      u64 want = (1ull << NFS) - 1;
      for (;;) {
        POLL();
        if (want >> lane & 1) zv = cl4(rw, lane * (2 * DM * 4), W_Z + oidx * 4);
        const u64 bad = __builtin_amdgcn_ballot_w64(!valid4(zv));
        if (bad == 0) break;
        if (bad & want) { want = bad & (0 - bad); __builtin_amdgcn_s_sleep(64); } else want = bad;
      }
      const u32x2 xv = *reinterpret_cast<const u32x2*>(x + oidx);
#pragma unroll
      for (int k = 0; k < 4; ++k) { const float f = rbf(wave_sum(asf(zv[k]))); gt[k] = rbf(f / (1.0f + __expf(-f))); }
      xo[0] = bflo(xv[0]); xo[1] = bfhi(xv[0]); xo[2] = bflo(xv[1]); xo[3] = bfhi(xv[1]);
    }
    STAMP(8);
    __builtin_amdgcn_s_setprio(3);
    BAR();
    float oM, oL, o0 = 0.f, o1 = 0.f;
    {
      f32x4 ml[4];
#pragma unroll
      for (int i = 0; i < 4; ++i) ml[i] = *reinterpret_cast<const f32x4*>(lds + L_MRG + i * 4);
      oM = fmaxf(fmaxf(fmaxf(ml[0][0], ml[0][2]), fmaxf(ml[1][0], ml[1][2])), fmaxf(fmaxf(ml[2][0], ml[2][2]), fmaxf(ml[3][0], ml[3][2])));
      oL = 0.f;
#pragma unroll
      for (int w = 0; w < NW; ++w) {
        const float wt = __builtin_amdgcn_exp2f(ml[w >> 1][(w & 1) * 2] - oM);
        oL = fmaf(wt, ml[w >> 1][(w & 1) * 2 + 1], oL);
        const f32x2 pv = *reinterpret_cast<const f32x2*>(lds + L_QP + w * DH + lane * 2);
        o0 = fmaf(wt, pv[0], o0); o1 = fmaf(wt, pv[1], o1);
      }
    }
    cs4(rw, l16, W_O1 + bid * 1024, u32x4{canon(o0), canon(o1), canon(oM), canon(oL)});
    STAMP(2);
    miss = 0xffu & ~(1u << j);
    for (;;) {
      POLL();
#pragma unroll
      for (int jp = 0; jp < 8; ++jp) if (miss >> jp & 1) g[jp] = cl4(rw, l16, W_O1 + wgid(h, jp) * 1024);
#pragma unroll
      for (int jp = 0; jp < 8; ++jp)
        if ((miss >> jp & 1) && __builtin_amdgcn_ballot_w64(!valid4(g[jp])) == 0) miss &= ~(1u << jp);
      if (miss == 0) break;
      if (__builtin_popcount(miss) >= BK1) __builtin_amdgcn_s_sleep(SL1);
    }
    STAMP(7);
#pragma unroll
    for (int bb = 0; bb < 2; ++bb) {
      float pm[4], pl[4], p0[4], p1[4];
#pragma unroll
      for (int s = 0; s < 4; ++s) {
        const int jp = bb * 4 + s; const bool own = jp == j;
        p0[s] = own ? o0 : asf(g[jp][0]); p1[s] = own ? o1 : asf(g[jp][1]); pm[s] = own ? oM : asf(g[jp][2]); pl[s] = own ? oL : asf(g[jp][3]);
      }
      const float Mb = fmaxf(fmaxf(pm[0], pm[1]), fmaxf(pm[2], pm[3]));
      float Lb = 0.f, a0 = 0.f, a1 = 0.f;
#pragma unroll
      for (int s = 0; s < 4; ++s) { const float wt = __builtin_amdgcn_exp2f(pm[s] - Mb); Lb = fmaf(wt, pl[s], Lb); a0 = fmaf(wt, p0[s], a0); a1 = fmaf(wt, p1[s], a1); }
      const float inv = 1.0f / Lb;
      reinterpret_cast<u32*>(lds)[L_OP + bb * 64 + lane] = cvtpk(a0 * inv, a1 * inv);
    }
    WAITL();
    float y[2][2] = {{0.f, 0.f}, {0.f, 0.f}};
#pragma unroll
    for (int k4 = 0; k4 < 8; ++k4)
#pragma unroll
      for (int bb = 0; bb < 2; ++bb) {
        const u32x4 ov = *reinterpret_cast<const u32x4*>(lds + L_OP + bb * 64 + (lane >> 5) * 32 + k4 * 4);
#pragma unroll
        for (int k = 0; k < 4; ++k) { y[bb][0] = dot2(P0[k4 * 4 + k], ov[k], y[bb][0]); y[bb][1] = dot2(P1[k4 * 4 + k], ov[k], y[bb][1]); }
      }
    {
      auto s0 = __builtin_amdgcn_permlane32_swap(__float_as_uint(y[0][0]), __float_as_uint(y[1][0]), false, false);
      auto s1 = __builtin_amdgcn_permlane32_swap(__float_as_uint(y[0][1]), __float_as_uint(y[1][1]), false, false);
      cs2(rw, l8, W_O2 + bid * 512, u32x2{canon(__uint_as_float(s0[0]) + __uint_as_float(s0[1])), canon(__uint_as_float(s1[0]) + __uint_as_float(s1[1]))});
    }
    u32x4 yv = {0, 0, 0, 0};
    {
      const u32 vo = wgid(lane & 31, j) * 512;
      bool need = lane < 32;
      for (;;) {
        POLL();
        if (need) { yv = cl4(rw, vo, W_O2 + h * 16); need = !valid4(yv); }
        const u64 bad = __builtin_amdgcn_ballot_w64(need);
        if (bad == 0) break;
        if (__builtin_popcountll(bad) >= BK2) __builtin_amdgcn_s_sleep(SL2);
      }
    }
    {
      float s[4] = {asf(yv[0]), asf(yv[1]), asf(yv[2]), asf(yv[3])};
#pragma unroll
      for (int k = 0; k < 4; ++k) {
        float t = row16_sum(s[k]);
        auto sw = __builtin_amdgcn_permlane16_swap(__float_as_uint(t), __float_as_uint(t), false, false);
        t = __uint_as_float(sw[0]) + __uint_as_float(sw[1]);
        s[k] = xo[k] + rbf(rbf(t) * gt[k]);
      }
      if (lane == 0) *reinterpret_cast<u32x2*>(out + oidx) = u32x2{cvtpk(s[0], s[1]), cvtpk(s[2], s[3])};
    }
    STAMP(3);
    return;
  }

  const int sw = wave - 1;
  const int t0 = fgrp ? (sp == 0 ? 0 : (256 - 3 * FD) + (256 + FD) * (sp - 1)) : 256 * sp;
  const u32 ntile = fgrp ? (sp == 0 ? 256u - 3 * FD : 256u + FD) : 256u;
  const size_t kvbase = ((size_t)(b * NH + h) * SK + (size_t)t0 * TILE) * DH;
  const char* kp = reinterpret_cast<const char*>(kc + kvbase);
  const char* vp = reinterpret_cast<const char*>(vc + kvbase);
  const u32 rot = (u32)((bid >> 3) * 8) % ntile;
  auto toff = [&](u32 t) -> size_t { u32 u = t + rot; if (u >= ntile) u -= ntile; return (size_t)u * 4096; };
  const u32 voff = lane * 16;
  const u32 ring = (u32)(uintptr_t)(__attribute__((address_space(3))) float*)lds + sw * RING_W;
  __builtin_amdgcn_s_barrier();
  u32x4 R[16];
  dma4k(kp + toff(sw), voff, ring);
  dma4k(vp + toff(sw), voff, ring + 4096);
  WAITV(4); dma4k(kp + toff((NW + sw)), voff, ring + 8192);
  WAITV(4); dma4k(vp + toff((NW + sw)), voff, ring + 12288);
  WAITV(4); ld4k(R, kp + toff((2 * NW + sw)), voff);
  WAITV(4); ld4k(R + 4, vp + toff((2 * NW + sw)), voff);
  WAITV(4); ld4k(R + 8, kp + toff((3 * NW + sw)), voff);
  WAITV(4); ld4k(R + 12, vp + toff((3 * NW + sw)), voff);
  for (;;) {
    FENCE();
    if (__builtin_amdgcn_readfirstlane(__hip_atomic_load(reinterpret_cast<u32*>(lds) + L_TK + 1, __ATOMIC_RELAXED, __HIP_MEMORY_SCOPE_WORKGROUP)) != 0) break;
    __builtin_amdgcn_s_sleep(2);
  }
  FENCE();
  const u32x4 qpk = *reinterpret_cast<const u32x4*>(lds + L_QK + 4 * c);

  constexpr float SCALE = 0.08838834764831845f;
  float m = -INFINITY, l = 0.f, acc[8];
#pragma unroll
  for (int k = 0; k < 8; ++k) acc[k] = 0.f;
  const bool b0 = (c & 1) != 0, b1 = (c & 2) != 0;
  const char* lbase = reinterpret_cast<const char*>(lds) + sw * RING_W + lane * 16;
  float p0, p1, p2, p3;

  auto score = [&](const u32x4* K) __attribute__((always_inline)) {
    float d[4];
#pragma unroll
    for (int t = 0; t < 4; ++t) {
      float s = dot2(K[t][0], qpk[0], 0.f);
      s = dot2(K[t][1], qpk[1], s); s = dot2(K[t][2], qpk[2], s); s = dot2(K[t][3], qpk[3], s);
      d[t] = s;
    }
    float e0 = b0 ? d[1] : d[0], f0 = b0 ? d[0] : d[1];
    float e1 = b0 ? d[3] : d[2], f1 = b0 ? d[2] : d[3];
    e0 += dpp<XOR1>(f0); e1 += dpp<XOR1>(f1);
    float g0 = b1 ? e1 : e0, g1 = b1 ? e0 : e1;
    g0 += dpp<XOR2>(g1);
    g0 += dpp<ROR4>(g0); g0 += dpp<XOR8>(g0);
    const float sc = rbf(rbf(g0) * SCALE) * LOG2E;
    float tm = fmaxf(sc, dpp<XOR1>(sc)); tm = fmaxf(tm, dpp<XOR2>(tm));
    const float mn = fmaxf(m, tm);
    const float alpha = __builtin_amdgcn_exp2f(m - mn), p = __builtin_amdgcn_exp2f(sc - mn);
    m = mn;
    l = fmaf(l, alpha, p);
    p0 = dpp<0x00>(p); p1 = dpp<0x55>(p); p2 = dpp<0xAA>(p); p3 = dpp<0xFF>(p);
#pragma unroll
    for (int k = 0; k < 8; ++k) acc[k] *= alpha;
  };
  auto accum = [&](const u32x4* V) __attribute__((always_inline)) {
#pragma unroll
    for (int i = 0; i < 4; ++i) {
      const float pt = i == 0 ? p0 : i == 1 ? p1 : i == 2 ? p2 : p3;
#pragma unroll
      for (int k = 0; k < 4; ++k) { acc[2 * k] = fmaf(pt, bflo(V[i][k]), acc[2 * k]); acc[2 * k + 1] = fmaf(pt, bfhi(V[i][k]), acc[2 * k + 1]); }
    }
  };
  auto rd = [&](u32x4* D, u32 off) __attribute__((always_inline)) {
#pragma unroll
    for (int i = 0; i < 4; ++i) D[i] = *reinterpret_cast<const u32x4*>(lbase + off + i * 1024);
  };
  u32* tkp = reinterpret_cast<u32*>(lds) + L_TK;
  auto ticket = [&]() -> u32 { u32 t = 0; if (lane == 0) t = __hip_atomic_fetch_add(tkp, 1u, __ATOMIC_RELAXED, __HIP_MEMORY_SCOPE_WORKGROUP); return t; };

  u32 slot = 0;
  u32 tk = ticket();
#pragma unroll
  for (int s2 = 0; s2 < 2; ++s2) {
    u32x4 D[4];
    WAITV(28);
    rd(D, s2 * 8192);
    WAITL();
    dma4k(kp + toff(((4 + s2) * NW + sw)), voff, ring + s2 * 8192);
    score(D);
    WAITV(28);
    rd(D, s2 * 8192 + 4096);
    WAITL();
    dma4k(vp + toff(((4 + s2) * NW + sw)), voff, ring + s2 * 8192 + 4096);
    accum(D);
  }
  PIN4(28, R); score(R);
  PIN4(24, R + 4); accum(R + 4);
  PIN4(20, R + 8); score(R + 8);
  PIN4(16, R + 12); accum(R + 12);
  for (;;) {
    const u32 t = __builtin_amdgcn_readfirstlane(tk);
    if (t >= ntile) break;
    tk = ticket();
    u32x4 D[4];
    WAITD();
    rd(D, slot);
    WAITL();
    dma4k(kp + toff(t), voff, ring + slot);
    score(D);
    WAITD();
    rd(D, slot + 4096);
    WAITL();
    dma4k(vp + toff(t), voff, ring + slot + 4096);
    accum(D);
    slot ^= 8192;
  }
  {
    u32x4 D[4];
    WAITV(12); rd(D, slot); score(D);
    WAITV(8); rd(D, slot + 4096); accum(D);
    WAITV(4); rd(D, slot ^ 8192); score(D);
    WAITV(0); rd(D, (slot ^ 8192) + 4096); accum(D);
  }
  l += dpp<XOR1>(l); l += dpp<XOR2>(l);
  {
    const float mw = xrow_max(m);
    const float sc = __builtin_amdgcn_exp2f(m - mw);
    l = xrow_sum(l * sc);
#pragma unroll
    for (int k = 0; k < 8; ++k) acc[k] = xrow_sum(acc[k] * sc);
    if (r == 0) {
      *reinterpret_cast<f32x4*>(lds + L_QP + sw * DH + c * 8) = f32x4{acc[0], acc[1], acc[2], acc[3]};
      *reinterpret_cast<f32x4*>(lds + L_QP + sw * DH + c * 8 + 4) = f32x4{acc[4], acc[5], acc[6], acc[7]};
      if (c == 0) { lds[L_MRG + sw * 2] = mw; lds[L_MRG + sw * 2 + 1] = l; }
    }
  }
  if (sw == 0) STAMP(9);
  if (sw == 7) STAMP(11);
  BAR();
}

extern "C" void kernel_launch(void* const* d_in, const int* in_sizes, int n_in, void* d_out, int out_size, void* d_ws, size_t ws_size,
                              hipStream_t stream) {
  const __hip_bfloat16* x = (const __hip_bfloat16*)d_in[0];
  const __hip_bfloat16* kc = (const __hip_bfloat16*)d_in[1];
  const __hip_bfloat16* vc = (const __hip_bfloat16*)d_in[2];
  const __hip_bfloat16* wq = (const __hip_bfloat16*)d_in[3];
  const __hip_bfloat16* wo = (const __hip_bfloat16*)d_in[4];
  const __hip_bfloat16* wg = (const __hip_bfloat16*)d_in[6];
  const __hip_bfloat16* wu = (const __hip_bfloat16*)d_in[7];
  const __hip_bfloat16* wd = (const __hip_bfloat16*)d_in[8];
  (void)hipFuncSetAttribute((const void*)fused_k, hipFuncAttributeMaxDynamicSharedMemorySize, L_END * 4);
  fused_k<<<NWG, NT, L_END * 4, stream>>>(x, kc, vc, wq, wo, wg, wu, wd, (char*)d_ws, (__hip_bfloat16*)d_out);
}
```
